```python
import torch

def _softmax(x, axis=-1):
    return torch.softmax(x, dim=axis)
def _gelu(x):
    return torch.nn.functional.gelu(x, approximate="tanh")
def _silu(x):
    return torch.nn.functional.silu(x)
def _sigmoid(x):
    return torch.sigmoid(x)
def _layernorm(x, axis=-1, eps=1e-5):
    m = x.mean(dim=axis, keepdim=True)
    v = x.var(dim=axis, keepdim=True, unbiased=False)
    return (x - m) / torch.sqrt(v + eps)
def _scatter_add(tgt, idx, src, axis=0):
    return tgt.clone().index_add_(axis, idx, src)
def _segment_sum(src, seg, n):
    out = torch.zeros((n,) + src.shape[1:], dtype=src.dtype)
    return out.index_add_(0, seg, src)
def _pad(x, pw):
    flat = tuple(v for pair in reversed(pw) for v in pair)
    return torch.nn.functional.pad(x, flat)

kernel_name = 'standalone__gqa_softmax__decode'

def setup_inputs():

    _w = (lambda a: (lambda t: t.to(torch.bfloat16) if t.is_floating_point() else t)(torch.as_tensor(a)))
    B, Hq, Hkv, Sq, Sk, D = 4, 32, 16, 1, 4096, 128
    return {
        "q": _w((lambda *s: torch.randn(*s, dtype=torch.bfloat16) * 0.8)(B, Hq, Sq, D)),
        "k": _w((lambda *s: torch.randn(*s, dtype=torch.bfloat16) * 0.8)(B, Hkv, Sk, D)),
        "v": _w((lambda *s: torch.randn(*s, dtype=torch.bfloat16) * 0.05)(B, Hkv, Sk, D)),
    }

def reference(q, k, v):
    B, Hq, Sq, D = q.shape
    Hkv = k.shape[1]
    rep = Hq // Hkv
    k = (lambda x, n, axis: torch.repeat_interleave(x, n, dim=axis))(k, rep, axis=1)
    v = (lambda x, n, axis: torch.repeat_interleave(x, n, dim=axis))(v, rep, axis=1)
    scale = D ** -0.5
    scores = torch.einsum("bhqd,bhkd->bhqk", q, k) * scale
    p = _softmax(scores, axis=-1)
    return torch.einsum("bhqk,bhkd->bhqd", p, v)
```

```cpp
#define K_STAMP 1
#define K_STAT -1
#define K_SCALE 1
#include <hip/hip_runtime.h>
#include <hip/hip_bf16.h>
#include <stdint.h>

#define K_WIN 16
#define K_XCDMAP 1
#define K_LAY4 0
#define K_LDSPAD 1

typedef __attribute__((ext_vector_type(4))) unsigned u32x4;
typedef __attribute__((ext_vector_type(4))) float f32x4;
typedef __attribute__((ext_vector_type(2))) float f32x2;
typedef __attribute__((ext_vector_type(8))) __bf16 bf16x8;
typedef __attribute__((ext_vector_type(2))) __bf16 bf16x2;
typedef __amdgpu_buffer_rsrc_t rsrc_t;

constexpr int NB = 4, HKV = 16, SK = 4096, D = 128;
constexpr int NGRP = NB * HKV;
constexpr int NSPLIT = 4, WAVES = 4, NTHR = WAVES * 64;
constexpr int KPS = SK / NSPLIT;
constexpr int NLW = KPS / 4 / WAVES;
constexpr int WIN = K_WIN;
constexpr int NIT = NLW / WIN, NT = WIN / 4;
constexpr float C2 = 0.08838834764831845f * 1.4426950408889634f;
constexpr int ITB = WIN * WAVES * 1024;

constexpr size_t WS_EPOCH = 0;
constexpr size_t WS_PART = 64 * 1024;

#define STAMP_OFF (8u << 20)
#define STAMP_NW (NGRP * NSPLIT * WAVES)
#define STAMP_N 5
#define STAMP(i) do { __builtin_amdgcn_sched_barrier(0); unsigned long long t_ = __builtin_amdgcn_s_memrealtime(); stampv[i] = t_; __builtin_amdgcn_sched_barrier(0); } while (0)
#define STAMP_FLUSH() do { if (lane == 0) { unsigned long long* sp_ = (unsigned long long*)(ws + STAMP_OFF) + ((size_t)blockIdx.x * WAVES + wave) * 8; for (int i_ = 0; i_ < STAMP_N; ++i_) sp_[i_] = stampv[i_]; } } while (0)

__device__ __forceinline__ float dot2(unsigned a, unsigned b, float acc) {
  return __builtin_amdgcn_fdot2_f32_bf16(__builtin_bit_cast(bf16x2, a), __builtin_bit_cast(bf16x2, b), acc, false);
}
template <int CTRL> __device__ __forceinline__ float dpp(float x) {
  return __builtin_bit_cast(float, __builtin_amdgcn_mov_dpp(__builtin_bit_cast(int, x), CTRL, 0xf, 0xf, true));
}
constexpr int XOR1 = 0xB1, XOR2 = 0x4E, ROR4 = 0x124, ROR8 = 0x128, ROR12 = 0x12C, IDENT = 0xE4;
__device__ __forceinline__ float xrow_max(float x) {
  auto s = __builtin_amdgcn_permlane16_swap(__float_as_uint(x), __float_as_uint(x), false, false);
  x = fmaxf(__uint_as_float(s[0]), __uint_as_float(s[1]));
  auto t = __builtin_amdgcn_permlane32_swap(__float_as_uint(x), __float_as_uint(x), false, false);
  return fmaxf(__uint_as_float(t[0]), __uint_as_float(t[1]));
}
__device__ __forceinline__ float xrow_sum(float x) {
  auto s = __builtin_amdgcn_permlane16_swap(__float_as_uint(x), __float_as_uint(x), false, false);
  x = __uint_as_float(s[0]) + __uint_as_float(s[1]);
  auto t = __builtin_amdgcn_permlane32_swap(__float_as_uint(x), __float_as_uint(x), false, false);
  return __uint_as_float(t[0]) + __uint_as_float(t[1]);
}
__device__ __forceinline__ rsrc_t mkrsrc(const unsigned char* p) {
  return __builtin_amdgcn_make_buffer_rsrc((void*)p, 0, ITB, 0x00020000);
}
__device__ __forceinline__ u32x4 ldstream(rsrc_t r, int voff, int u) {
  u32x4 v = __builtin_amdgcn_raw_buffer_load_b128(r, voff, u * 4096, 2);
  asm volatile("" ::: "memory");
  __builtin_amdgcn_sched_barrier(0);
  return v;
}
template <int R, int B> __device__ __forceinline__ unsigned place(unsigned old, unsigned src) {
  constexpr int n = (R - B) & 3;
  constexpr int ctrl = n == 0 ? IDENT : n == 1 ? ROR4 : n == 2 ? ROR8 : ROR12;
  return (unsigned)__builtin_amdgcn_update_dpp((int)old, (int)src, ctrl, 0xf, 1 << R, false);
}
template <int B> __device__ __forceinline__ void place4(u32x4& a, unsigned src) {
  a[0] = place<0, B>(a[0], src); a[1] = place<1, B>(a[1], src); a[2] = place<2, B>(a[2], src); a[3] = place<3, B>(a[3], src);
}
__device__ __forceinline__ f32x4 mfma(u32x4 a, u32x4 b, f32x4 c) {
  return __builtin_amdgcn_mfma_f32_16x16x32_bf16(__builtin_bit_cast(bf16x8, a), __builtin_bit_cast(bf16x8, b), c, 0, 0, 0);
}

constexpr int LDS_T = 0;
constexpr int LDS_O = WAVES * (NLW / 4) * 64;
constexpr int LDS_M = LDS_O + WAVES * 256;
constexpr int LDS_L = LDS_M + 8;
constexpr int LDS_N = K_LDSPAD ? 21 * 1024 : LDS_L + 8;

__global__ void __launch_bounds__(NTHR)
gqa_decode_kernel(const u32x4* __restrict__ q, const unsigned char* kc, const unsigned char* vc,
                  unsigned short* __restrict__ out, unsigned char* __restrict__ ws) {
  __shared__ __attribute__((aligned(16))) float lds[LDS_N];
  unsigned long long stampv[STAMP_N] = {0, 0, 0, 0, 0};
  STAMP(0);
  const int tid = threadIdx.x, lane = tid & 63;
  const int wave = __builtin_amdgcn_readfirstlane(tid >> 6);
  const int lin = (blockIdx.x & 7) * (NGRP * NSPLIT / 8) + (blockIdx.x >> 3);
  const int grp = lin / NSPLIT, split = lin % NSPLIT;
  const int c = lane & 15, hl = (lane >> 1) & 1;

  const u32x4 qa = q[grp * 32 + hl * 16 + c];
  const u32x4 qb = q[grp * 32 + (hl ^ 1) * 16 + c];
  unsigned* ep = reinterpret_cast<unsigned*>(ws + WS_EPOCH + ((size_t)grp * 4 + wave) * 128);
  const unsigned epoch = __hip_atomic_load(ep, __ATOMIC_RELAXED, __HIP_MEMORY_SCOPE_AGENT);

  const size_t base = (size_t)lin * (KPS * D * 2);
  const unsigned char* kp = kc + base;
  const unsigned char* vp = vc + base;
  const int voff = lane * 16 + wave * (K_LAY4 ? 4096 : 1024);

  u32x4 ring[WIN];
  {
    const rsrc_t r = mkrsrc(kp);
#pragma unroll
    for (int u = 0; u < WIN; ++u) ring[u] = ldstream(r, voff, u);
  }
  float* sT = lds + LDS_T + wave * (NLW / 4) * 64;
  float mx = -INFINITY;
  const bool b3 = lane & 8, b2 = lane & 4;
#pragma unroll 1
  for (int it = 0; it < NIT; ++it) {
    const rsrc_t r = mkrsrc(it + 1 < NIT ? kp + (size_t)(it + 1) * ITB : vp);
#pragma unroll
    for (int a = 0; a < NT; ++a) {
      float z[4];
#pragma unroll
      for (int b = 0; b < 4; ++b) {
        const int u = a * 4 + b;
        float da = 0.f, db = 0.f;
#pragma unroll
        for (int k = 0; k < 4; ++k) { da = dot2(ring[u][k], qa[k], da); db = dot2(ring[u][k], qb[k], db); }
        __builtin_amdgcn_sched_barrier(0);
        ring[u] = ldstream(r, voff, u);
        z[b] = da + dpp<XOR2>(db);
      }
      const float u0 = z[0] + dpp<ROR8>(z[0]), u2 = z[2] + dpp<ROR8>(z[2]);
      const float u1 = z[1] + dpp<ROR8>(z[1]), u3 = z[3] + dpp<ROR8>(z[3]);
      const float w0 = b3 ? u2 : u0, w1 = b3 ? u3 : u1;
      const float x0 = w0 + dpp<0x104>(w0);
      const float x1 = w1 + dpp<0x114>(w1);
      float t = b2 ? x1 : x0;
      t += dpp<XOR1>(t);
      mx = fmaxf(mx, t);
      sT[(it * NT + a) * 64 + lane] = t;
      __builtin_amdgcn_sched_barrier(0);
    }
  }
  STAMP(1);
  mx = fmaxf(mx, dpp<ROR4>(mx)); mx = fmaxf(mx, dpp<ROR8>(mx)); mx = xrow_max(mx);
  if ((lane & 61) == 0) lds[LDS_M + hl * 4 + wave] = mx;
  __syncthreads();
  {
    const f32x4 t = *reinterpret_cast<const f32x4*>(lds + LDS_M + hl * 4);
    mx = fmaxf(fmaxf(t[0], t[1]), fmaxf(t[2], t[3]));
  }
  const float mc = mx * C2;
  {
    const unsigned hm = (lane & 1) ? 0xffff0000u : 0x0000ffffu;
    float l = 0.f;
#pragma unroll
    for (int i = 0; i < NLW / 4; ++i) {
      const float p = __builtin_amdgcn_exp2f(fmaf(sT[i * 64 + lane], C2, -mc));
      l += p;
      bf16x2 b; b[0] = (__bf16)p; b[1] = b[0];
      sT[i * 64 + lane] = __uint_as_float(__builtin_bit_cast(unsigned, b) & hm);
    }
    l += dpp<ROR4>(l); l += dpp<ROR8>(l); l = xrow_sum(l);
    if ((lane & 61) == 0) lds[LDS_L + hl * 4 + wave] = l;
  }

  f32x4 acc = {0.f, 0.f, 0.f, 0.f};
  u32x4 A0 = {0u, 0u, 0u, 0u}, A1 = {0u, 0u, 0u, 0u};
#pragma unroll 1
  for (int it = 0; it < NIT; ++it) {
    const rsrc_t r = __builtin_amdgcn_make_buffer_rsrc((void*)(vp + (size_t)((it + 1) & (NIT - 1)) * ITB), 0, it + 1 < NIT ? ITB : 0, 0x00020000);
    unsigned P[NT];
#pragma unroll
    for (int a = 0; a < NT; ++a) P[a] = __float_as_uint(sT[(it * NT + a) * 64 + lane]);
#pragma unroll
    for (int a = 0; a < NT; ++a) {
      place4<0>(A0, P[a]); acc = mfma(A0, ring[a * 4 + 0], acc); __builtin_amdgcn_sched_barrier(0);
      ring[a * 4 + 0] = ldstream(r, voff, a * 4 + 0);
      place4<1>(A1, P[a]); acc = mfma(A1, ring[a * 4 + 1], acc); __builtin_amdgcn_sched_barrier(0);
      ring[a * 4 + 1] = ldstream(r, voff, a * 4 + 1);
      place4<2>(A0, P[a]); acc = mfma(A0, ring[a * 4 + 2], acc); __builtin_amdgcn_sched_barrier(0);
      ring[a * 4 + 2] = ldstream(r, voff, a * 4 + 2);
      place4<3>(A1, P[a]); acc = mfma(A1, ring[a * 4 + 3], acc); __builtin_amdgcn_sched_barrier(0);
      ring[a * 4 + 3] = ldstream(r, voff, a * 4 + 3);
    }
  }
  {
    float* so = lds + LDS_O + wave * 256 + c * 8 + (lane >> 4) * 2;
    *reinterpret_cast<f32x2*>(so) = f32x2{acc[0], acc[1]};
    *reinterpret_cast<f32x2*>(so + 128) = f32x2{acc[2], acc[3]};
  }
  STAMP(2);
  __syncthreads();

  const float o = (lds[LDS_O + tid] + lds[LDS_O + 256 + tid]) + (lds[LDS_O + 512 + tid] + lds[LDS_O + 768 + tid]);
  const f32x4 tl = *reinterpret_cast<const f32x4*>(lds + LDS_L + (tid >> 7) * 4);
  const f32x4 tm = *reinterpret_cast<const f32x4*>(lds + LDS_M + (tid >> 7) * 4);
  const float L = (tl[0] + tl[1]) + (tl[2] + tl[3]);
  const float M = fmaxf(fmaxf(tm[0], tm[1]), fmaxf(tm[2], tm[3]));
  const float on = o * __builtin_amdgcn_rcpf(L);
  const float lse = fmaf(M, C2, __builtin_amdgcn_logf(L));
  u32x4* part = reinterpret_cast<u32x4*>(ws + WS_PART) + (size_t)grp * NSPLIT * 256 + tid;
  const unsigned tag = epoch + 0x9E3779B9u;
  if (split != 0) {
    u32x4 rec = {tag, __float_as_uint(on), tag, __float_as_uint(lse)};
    asm volatile("global_store_dwordx4 %0, %1, off sc1\n\ts_nop 1" :: "v"(part + split * 256), "v"(rec) : "memory");
    STAMP(3); STAMP_FLUSH();
    return;
  }
  STAMP(3);
  u32x4 r[NSPLIT - 1];
  for (unsigned spin = 0; spin < (1u << 20); ++spin) {
#pragma unroll
    for (int s = 1; s < NSPLIT; ++s)
      asm volatile("global_load_dwordx4 %0, %1, off sc1" : "=v"(r[s - 1]) : "v"(part + s * 256) : "memory");
    bool ok = true;
#pragma unroll
    for (int s = 1; s < NSPLIT; ++s) {
      asm volatile("s_waitcnt vmcnt(0)" : "+v"(r[s - 1]) :: "memory");
      ok &= (r[s - 1][0] == tag) & (r[s - 1][2] == tag);
    }
    if (__all(ok)) break;
    __builtin_amdgcn_s_sleep(1);
  }
  float Mx = lse;
#pragma unroll
  for (int s = 1; s < NSPLIT; ++s) Mx = fmaxf(Mx, __uint_as_float(r[s - 1][3]));
  float wsum = __builtin_amdgcn_exp2f(lse - Mx), osum = wsum * on;
#pragma unroll
  for (int s = 1; s < NSPLIT; ++s) {
    const float w = __builtin_amdgcn_exp2f(__uint_as_float(r[s - 1][3]) - Mx);
    wsum += w; osum = fmaf(w, __uint_as_float(r[s - 1][1]), osum);
  }
  out[grp * 256 + tid] = __builtin_bit_cast(unsigned short, (__bf16)(osum * __builtin_amdgcn_rcpf(wsum)));
  if (lane == 0) __hip_atomic_store(ep, tag, __ATOMIC_RELAXED, __HIP_MEMORY_SCOPE_AGENT);
  STAMP(4); STAMP_FLUSH();
}

__global__ void gqa_stat_kernel(unsigned char* ws) {
  const unsigned long long* sp = (const unsigned long long*)(ws + STAMP_OFF);
  const unsigned ref = (unsigned)sp[0] - 100000u;
  unsigned mn0 = ~0u, mx = 0, mn = ~0u;
  constexpr int PH = K_STAT < 0 ? 0 : (K_STAT % 10);
  for (int i = threadIdx.x; i < STAMP_NW; i += 256) {
    unsigned a = (unsigned)sp[8 * i] - ref; unsigned long long braw = sp[8 * i + PH]; unsigned b = (unsigned)braw - ref;
    mn0 = min(mn0, a); if (braw) { mx = max(mx, b); mn = min(mn, b); }
  }
  for (int o = 32; o; o >>= 1) { mn0 = min(mn0, (unsigned)__shfl_xor(mn0, o)); mx = max(mx, (unsigned)__shfl_xor(mx, o)); mn = min(mn, (unsigned)__shfl_xor(mn, o)); }
  __shared__ unsigned sh[12];
  if ((threadIdx.x & 63) == 0) { sh[threadIdx.x >> 6] = mn0; sh[4 + (threadIdx.x >> 6)] = mx; sh[8 + (threadIdx.x >> 6)] = mn; }
  __syncthreads();
  mn0 = min(min(sh[0], sh[1]), min(sh[2], sh[3])); mx = max(max(sh[4], sh[5]), max(sh[6], sh[7])); mn = min(min(sh[8], sh[9]), min(sh[10], sh[11]));
  unsigned dl = K_STAT < 0 ? ((mx - mn0) >> 20) : K_STAT < 10 ? K_SCALE * (mx - mn0) : K_SCALE * (mn - mn0);
  if (dl > 100000) dl = 100000;
  const unsigned long long t0 = __builtin_amdgcn_s_memrealtime();
  while (__builtin_amdgcn_s_memrealtime() - t0 < dl) __builtin_amdgcn_s_sleep(2);
}
extern "C" void kernel_launch(void* const* d_in, const int* in_sizes, int n_in,
                              void* d_out, int out_size, void* d_ws, size_t ws_size,
                              hipStream_t stream) {
  (void)in_sizes; (void)n_in; (void)out_size; (void)ws_size;
  gqa_decode_kernel<<<NGRP * NSPLIT, NTHR, 0, stream>>>((const u32x4*)d_in[0], (const unsigned char*)d_in[1], (const unsigned char*)d_in[2],
                                                        (unsigned short*)d_out, (unsigned char*)d_ws);
  gqa_stat_kernel<<<1, 256, 0, stream>>>((unsigned char*)d_ws);
}
```

```cpp
#define K_WIN 16
#define K_GB 32
#define K_POLLA 3
#define K_RATE 7600
#define K_STAMP 1
#define K_STAT -1
#define K_SCALE 1
#include <hip/hip_runtime.h>
#include <hip/hip_bf16.h>
#include <stdint.h>

#define K_XCDMAP 1
#define K_G0 2
#define K_LEAD 50
#define K_LDSPAD 1

typedef __attribute__((ext_vector_type(4))) unsigned u32x4;
typedef __attribute__((ext_vector_type(4))) float f32x4;
typedef __attribute__((ext_vector_type(2))) float f32x2;
typedef __attribute__((ext_vector_type(8))) __bf16 bf16x8;
typedef __attribute__((ext_vector_type(2))) __bf16 bf16x2;
typedef __amdgpu_buffer_rsrc_t rsrc_t;

constexpr int NB = 4, HKV = 16, SK = 4096, D = 128;
constexpr int NGRP = NB * HKV;
constexpr int NSPLIT = 4, WAVES = 4, NTHR = WAVES * 64;
constexpr int KPS = SK / NSPLIT;
constexpr int NLW = KPS / 4 / WAVES;
constexpr int WIN = K_WIN;
constexpr int NIT = NLW / WIN, NT = WIN / 4;
constexpr float C2 = 0.08838834764831845f * 1.4426950408889634f;
constexpr int ITB = WIN * WAVES * 1024;
constexpr int NG = 2 * NLW / 4;
constexpr double STEPS_P = NG - K_G0, STEPS_C = K_GB - K_G0 + 1 < STEPS_P ? K_GB - K_G0 + 1 : STEPS_P;
constexpr double TAU_P = K_RATE ? (NGRP * (NSPLIT - 1) + NGRP * (STEPS_C / (STEPS_P + (K_GB < NG ? 1 : 0)))) * 16384.0 / (K_RATE * 1e9) * 1e8 * 256 : 0;
constexpr double TAU_C = TAU_P * (STEPS_P + (K_GB < NG ? 1 : 0)) / STEPS_C;

constexpr size_t WS_EPOCH = 0;
constexpr size_t WS_PART = 64 * 1024;

#define STAMP_OFF (8u << 20)
#define STAMP_NW (NGRP * NSPLIT * WAVES)
#define STAMP_N 7
#define STAMP(i) do { __builtin_amdgcn_sched_barrier(0); unsigned long long t_ = __builtin_amdgcn_s_memrealtime(); stampv[i] = t_; __builtin_amdgcn_sched_barrier(0); } while (0)
#define STAMP_FLUSH() do { if (lane == 0) { unsigned long long* sp_ = (unsigned long long*)(ws + STAMP_OFF) + ((size_t)blockIdx.x * WAVES + wave) * 8; for (int i_ = 0; i_ < STAMP_N; ++i_) sp_[i_] = stampv[i_]; } } while (0)

__device__ __forceinline__ float dot2(unsigned a, unsigned b, float acc) {
  return __builtin_amdgcn_fdot2_f32_bf16(__builtin_bit_cast(bf16x2, a), __builtin_bit_cast(bf16x2, b), acc, false);
}
template <int CTRL> __device__ __forceinline__ float dpp(float x) {
  return __builtin_bit_cast(float, __builtin_amdgcn_mov_dpp(__builtin_bit_cast(int, x), CTRL, 0xf, 0xf, true));
}
constexpr int XOR1 = 0xB1, XOR2 = 0x4E, ROR4 = 0x124, ROR8 = 0x128, ROR12 = 0x12C, IDENT = 0xE4;
__device__ __forceinline__ float xrow_max(float x) {
  auto s = __builtin_amdgcn_permlane16_swap(__float_as_uint(x), __float_as_uint(x), false, false);
  x = fmaxf(__uint_as_float(s[0]), __uint_as_float(s[1]));
  auto t = __builtin_amdgcn_permlane32_swap(__float_as_uint(x), __float_as_uint(x), false, false);
  return fmaxf(__uint_as_float(t[0]), __uint_as_float(t[1]));
}
__device__ __forceinline__ float xrow_sum(float x) {
  auto s = __builtin_amdgcn_permlane16_swap(__float_as_uint(x), __float_as_uint(x), false, false);
  x = __uint_as_float(s[0]) + __uint_as_float(s[1]);
  auto t = __builtin_amdgcn_permlane32_swap(__float_as_uint(x), __float_as_uint(x), false, false);
  return __uint_as_float(t[0]) + __uint_as_float(t[1]);
}
__device__ __forceinline__ rsrc_t mkrsrc(const unsigned char* p) {
  return __builtin_amdgcn_make_buffer_rsrc((void*)p, 0, ITB, 0x00020000);
}
__device__ __forceinline__ u32x4 ldstream(rsrc_t r, int voff, int u) {
  u32x4 v = __builtin_amdgcn_raw_buffer_load_b128(r, voff, u * 4096, 2);
  asm volatile("" ::: "memory");
  __builtin_amdgcn_sched_barrier(0);
  return v;
}
__device__ __forceinline__ void pace(unsigned& tgt, unsigned tau, unsigned tend) {
  asm volatile("s_cmp_eq_u32 %0, %2\n\t"
               "s_cbranch_scc1 2f\n\t"
               "s_add_u32 %0, %0, %1\n\t"
               "s_branch 1f\n"
               "0:\n\t"
               "s_sleep 1\n"
               "1:\n\t"
               "s_memrealtime s[96:97]\n\t"
               "s_waitcnt lgkmcnt(0)\n\t"
               "s_lshl_b32 s96, s96, 8\n\t"
               "s_sub_u32 s96, s96, %0\n\t"
               "s_cmp_lt_i32 s96, 0\n\t"
               "s_cbranch_scc1 0b\n"
               "2:" : "+s"(tgt) : "s"(tau), "s"(tend) : "s96", "s97", "scc", "memory");
  __builtin_amdgcn_sched_barrier(0);
}
template <int R, int B> __device__ __forceinline__ unsigned place(unsigned old, unsigned src) {
  constexpr int n = (R - B) & 3;
  constexpr int ctrl = n == 0 ? IDENT : n == 1 ? ROR4 : n == 2 ? ROR8 : ROR12;
  return (unsigned)__builtin_amdgcn_update_dpp((int)old, (int)src, ctrl, 0xf, 1 << R, false);
}
template <int B> __device__ __forceinline__ void place4(u32x4& a, unsigned src) {
  a[0] = place<0, B>(a[0], src); a[1] = place<1, B>(a[1], src); a[2] = place<2, B>(a[2], src); a[3] = place<3, B>(a[3], src);
}
__device__ __forceinline__ f32x4 mfma(u32x4 a, u32x4 b, f32x4 c) {
  c = __builtin_amdgcn_mfma_f32_16x16x32_bf16(__builtin_bit_cast(bf16x8, a), __builtin_bit_cast(bf16x8, b), c, 0, 0, 0);
  asm volatile("" : "+a"(c));
  return c;
}

constexpr int LDS_T = 0;
constexpr int LDS_O = WAVES * (NLW / 4) * 64;
constexpr int LDS_M = LDS_O + WAVES * 256;
constexpr int LDS_L = LDS_M + 8;
constexpr int LDS_N = K_LDSPAD ? 21 * 1024 : LDS_L + 8;

__global__ void __launch_bounds__(NTHR)
gqa_decode_kernel(const u32x4* __restrict__ q, const unsigned char* kc, const unsigned char* vc,
                  unsigned short* __restrict__ out, unsigned char* __restrict__ ws) {
  __shared__ __attribute__((aligned(16))) float lds[LDS_N];
  unsigned long long stampv[STAMP_N] = {};
  STAMP(0);
  const unsigned tbase = (unsigned)__builtin_amdgcn_s_memrealtime() + K_LEAD;
  const int tid = threadIdx.x, lane = tid & 63;
  const int wave = __builtin_amdgcn_readfirstlane(tid >> 6);
  const int lin = (blockIdx.x & 7) * (NGRP * NSPLIT / 8) + (blockIdx.x >> 3);
  const int grp = lin / NSPLIT, split = lin % NSPLIT;
  const int c = lane & 15, hl = (lane >> 1) & 1;

  const u32x4 qa = q[grp * 32 + hl * 16 + c];
  const u32x4 qb = q[grp * 32 + (hl ^ 1) * 16 + c];
  unsigned* ep = reinterpret_cast<unsigned*>(ws + WS_EPOCH + ((size_t)grp * 4 + wave) * 128);
  const unsigned epoch = __hip_atomic_load(ep, __ATOMIC_RELAXED, __HIP_MEMORY_SCOPE_AGENT);

  const size_t base = (size_t)lin * (KPS * D * 2);
  const unsigned char* kp = kc + base;
  const unsigned char* vp = vc + base;
  const int voff = lane * 16 + wave * 1024;

  const unsigned tau = split ? (unsigned)TAU_P : (unsigned)TAU_C;
  unsigned tgt = tbase << 8;
  const unsigned tend = tgt + (split ? (unsigned)TAU_P * (unsigned)STEPS_P : (unsigned)TAU_C * (unsigned)STEPS_C);
  u32x4 ring[WIN];
  {
    const rsrc_t r = mkrsrc(kp);
#pragma unroll
    for (int u = 0; u < WIN; ++u) {
      if (u == K_G0 * 4) { asm volatile("s_waitcnt vmcnt(7)" ::: "memory"); STAMP(5); asm volatile("s_waitcnt vmcnt(0)" ::: "memory"); STAMP(6); }
      if (u % 4 == 0 && u / 4 >= K_G0) pace(tgt, tau, tend);
      ring[u] = ldstream(r, voff, u);
    }
  }
  float* sT = lds + LDS_T + wave * (NLW / 4) * 64;
  float mx = -INFINITY;
  const bool b3 = lane & 8, b2 = lane & 4;
#pragma unroll 1
  for (int it = 0; it < NIT; ++it) {
    const rsrc_t r = mkrsrc(it + 1 < NIT ? kp + (size_t)(it + 1) * ITB : vp);
#pragma unroll
    for (int a = 0; a < NT; ++a) {
      float z[4];
#pragma unroll
      for (int b = 0; b < 4; ++b) {
        const int u = a * 4 + b;
        float da = 0.f, db = 0.f;
#pragma unroll
        for (int k = 0; k < 4; ++k) { da = dot2(ring[u][k], qa[k], da); db = dot2(ring[u][k], qb[k], db); }
        asm volatile("" : "+v"(da), "+v"(db));
        __builtin_amdgcn_sched_barrier(0);
        if (b == 0) pace(tgt, tau, tend);
        ring[u] = ldstream(r, voff, u);
        z[b] = da + dpp<XOR2>(db);
      }
      const float u0 = z[0] + dpp<ROR8>(z[0]), u2 = z[2] + dpp<ROR8>(z[2]);
      const float u1 = z[1] + dpp<ROR8>(z[1]), u3 = z[3] + dpp<ROR8>(z[3]);
      const float w0 = b3 ? u2 : u0, w1 = b3 ? u3 : u1;
      const float x0 = w0 + dpp<0x104>(w0);
      const float x1 = w1 + dpp<0x114>(w1);
      float t = b2 ? x1 : x0;
      t += dpp<XOR1>(t);
      mx = fmaxf(mx, t);
      sT[(it * NT + a) * 64 + lane] = t;
      __builtin_amdgcn_sched_barrier(0);
    }
  }
  STAMP(1);
  mx = fmaxf(mx, dpp<ROR4>(mx)); mx = fmaxf(mx, dpp<ROR8>(mx)); mx = xrow_max(mx);
  const float mc = mx * C2;
  const unsigned hm = (lane & 1) ? 0xffff0000u : 0x0000ffffu;
  float l = 0.f;
  u32x4* part = reinterpret_cast<u32x4*>(ws + WS_PART) + (size_t)grp * NSPLIT * 256 + tid;
  u32x4 r[NSPLIT - 1] = {};

  f32x4 acc = {0.f, 0.f, 0.f, 0.f};
  u32x4 A0 = {0u, 0u, 0u, 0u}, A1 = {0u, 0u, 0u, 0u};
#pragma unroll 1
  for (int it = 0; it < NIT; ++it) {
    const rsrc_t rv = __builtin_amdgcn_make_buffer_rsrc((void*)(vp + (size_t)((it + 1) & (NIT - 1)) * ITB), 0, it + 1 < NIT ? ITB : 0, 0x00020000);
    const rsrc_t rp = __builtin_amdgcn_make_buffer_rsrc((void*)(ws + WS_PART + (size_t)grp * NSPLIT * 4096), 0, it + 1 == NIT && split == 0 ? NSPLIT * 4096 : 0, 0x00020000);
    unsigned P[NT];
#pragma unroll
    for (int a = 0; a < NT; ++a) {
      const float p = __builtin_amdgcn_exp2f(fmaf(sT[(it * NT + a) * 64 + lane], C2, -mc));
      l += p;
      bf16x2 b; b[0] = (__bf16)p; b[1] = b[0];
      P[a] = __builtin_bit_cast(unsigned, b) & hm;
    }
#pragma unroll
    for (int a = 0; a < NT; ++a) {
      place4<0>(A0, P[a]); acc = mfma(A0, ring[a * 4 + 0], acc); __builtin_amdgcn_sched_barrier(0);
      pace(tgt, tau, tend);
      if (a == K_POLLA % NT) {
#pragma unroll
        for (int s = 1; s < NSPLIT; ++s) {
          asm volatile("" :: "v"(r[s - 1]));
          r[s - 1] = __builtin_amdgcn_raw_buffer_load_b128(rp, voff, s * 4096, 16);
        }
        asm volatile("" ::: "memory");
        __builtin_amdgcn_sched_barrier(0);
      }
      ring[a * 4 + 0] = ldstream(rv, voff, a * 4 + 0);
      place4<1>(A1, P[a]); acc = mfma(A1, ring[a * 4 + 1], acc); __builtin_amdgcn_sched_barrier(0);
      ring[a * 4 + 1] = ldstream(rv, voff, a * 4 + 1);
      place4<2>(A0, P[a]); acc = mfma(A0, ring[a * 4 + 2], acc); __builtin_amdgcn_sched_barrier(0);
      ring[a * 4 + 2] = ldstream(rv, voff, a * 4 + 2);
      place4<3>(A1, P[a]); acc = mfma(A1, ring[a * 4 + 3], acc); __builtin_amdgcn_sched_barrier(0);
      ring[a * 4 + 3] = ldstream(rv, voff, a * 4 + 3);
    }
  }
  {
    float* so = lds + LDS_O + wave * 256 + c * 8 + (lane >> 4) * 2;
    *reinterpret_cast<f32x2*>(so) = f32x2{acc[0], acc[1]};
    *reinterpret_cast<f32x2*>(so + 128) = f32x2{acc[2], acc[3]};
    l += dpp<ROR4>(l); l += dpp<ROR8>(l); l = xrow_sum(l);
    if ((lane & 61) == 0) { lds[LDS_M + hl * 4 + wave] = mc; lds[LDS_L + hl * 4 + wave] = l; }
  }
  STAMP(2);
  __syncthreads();

  const f32x4 tl = *reinterpret_cast<const f32x4*>(lds + LDS_L + (tid >> 7) * 4);
  const f32x4 tm = *reinterpret_cast<const f32x4*>(lds + LDS_M + (tid >> 7) * 4);
  const float M = fmaxf(fmaxf(tm[0], tm[1]), fmaxf(tm[2], tm[3]));
  float o = 0.f, L = 0.f;
#pragma unroll
  for (int w = 0; w < WAVES; ++w) {
    const float f = __builtin_amdgcn_exp2f(tm[w] - M);
    o = fmaf(f, lds[LDS_O + w * 256 + tid], o); L = fmaf(f, tl[w], L);
  }
  const float on = o * __builtin_amdgcn_rcpf(L);
  const float lse = M + __builtin_amdgcn_logf(L);
  const unsigned tag = epoch + 0x9E3779B9u;
  if (split != 0) {
    u32x4 rec = {tag, __float_as_uint(on), tag, __float_as_uint(lse)};
    asm volatile("global_store_dwordx4 %0, %1, off sc1\n\ts_nop 1" :: "v"(part + split * 256), "v"(rec) : "memory");
    STAMP(3); STAMP_FLUSH();
    return;
  }
  STAMP(3);
  const rsrc_t rp = __builtin_amdgcn_make_buffer_rsrc((void*)(ws + WS_PART + (size_t)grp * NSPLIT * 4096), 0, NSPLIT * 4096, 0x00020000);
#pragma unroll 1
  for (unsigned spin = 0; spin < (1u << 20); ++spin) {
    bool ok = true;
#pragma unroll
    for (int s = 1; s < NSPLIT; ++s) ok &= (r[s - 1][0] == tag) & (r[s - 1][2] == tag);
    if (__all(ok)) break;
#pragma unroll
    for (int s = 1; s < NSPLIT; ++s) r[s - 1] = __builtin_amdgcn_raw_buffer_load_b128(rp, voff, s * 4096, 16);
    asm volatile("" ::: "memory");
  }
  float Mx = lse;
#pragma unroll
  for (int s = 1; s < NSPLIT; ++s) Mx = fmaxf(Mx, __uint_as_float(r[s - 1][3]));
  float wsum = __builtin_amdgcn_exp2f(lse - Mx), osum = wsum * on;
#pragma unroll
  for (int s = 1; s < NSPLIT; ++s) {
    const float w = __builtin_amdgcn_exp2f(__uint_as_float(r[s - 1][3]) - Mx);
    wsum += w; osum = fmaf(w, __uint_as_float(r[s - 1][1]), osum);
  }
  out[grp * 256 + tid] = __builtin_bit_cast(unsigned short, (__bf16)(osum * __builtin_amdgcn_rcpf(wsum)));
  if (lane == 0) __hip_atomic_store(ep, tag, __ATOMIC_RELAXED, __HIP_MEMORY_SCOPE_AGENT);
  STAMP(4); STAMP_FLUSH();
}

__global__ void gqa_stat_kernel(unsigned char* ws) {
  const unsigned long long* sp = (const unsigned long long*)(ws + STAMP_OFF);
  const unsigned ref = (unsigned)sp[0] - 100000u;
  unsigned mn0 = ~0u, mx = 0, mn = ~0u;
  constexpr int PH = K_STAT < 0 ? 0 : (K_STAT % 10);
  for (int i = threadIdx.x; i < STAMP_NW; i += 256) {
    unsigned a = (unsigned)sp[8 * i] - ref; unsigned long long braw = sp[8 * i + PH]; unsigned b = (unsigned)braw - ref;
    mn0 = min(mn0, a); if (braw) { mx = max(mx, b); mn = min(mn, b); }
  }
  for (int o = 32; o; o >>= 1) { mn0 = min(mn0, (unsigned)__shfl_xor(mn0, o)); mx = max(mx, (unsigned)__shfl_xor(mx, o)); mn = min(mn, (unsigned)__shfl_xor(mn, o)); }
  __shared__ unsigned sh[12];
  if ((threadIdx.x & 63) == 0) { sh[threadIdx.x >> 6] = mn0; sh[4 + (threadIdx.x >> 6)] = mx; sh[8 + (threadIdx.x >> 6)] = mn; }
  __syncthreads();
  mn0 = min(min(sh[0], sh[1]), min(sh[2], sh[3])); mx = max(max(sh[4], sh[5]), max(sh[6], sh[7])); mn = min(min(sh[8], sh[9]), min(sh[10], sh[11]));
  unsigned dl = K_STAT < 0 ? ((mx - mn0) >> 20) : K_STAT < 10 ? K_SCALE * (mx - mn0) : K_SCALE * (mn - mn0);
  if (dl > 100000) dl = 100000;
  const unsigned long long t0 = __builtin_amdgcn_s_memrealtime();
  while (__builtin_amdgcn_s_memrealtime() - t0 < dl) __builtin_amdgcn_s_sleep(2);
}
extern "C" void kernel_launch(void* const* d_in, const int* in_sizes, int n_in,
                              void* d_out, int out_size, void* d_ws, size_t ws_size,
                              hipStream_t stream) {
  (void)in_sizes; (void)n_in; (void)out_size; (void)ws_size;
  gqa_decode_kernel<<<NGRP * NSPLIT, NTHR, 0, stream>>>((const u32x4*)d_in[0], (const unsigned char*)d_in[1], (const unsigned char*)d_in[2],
                                                        (unsigned short*)d_out, (unsigned char*)d_ws);
  gqa_stat_kernel<<<1, 256, 0, stream>>>((unsigned char*)d_ws);
}
```

```cpp
#define K_WIN 16
#define K_GB 32
#define K_POLLA 3
#define K_RATE 7600
#define K_STAMP 1
#define K_STAT -1
#define K_SCALE 1
#include <hip/hip_runtime.h>
#include <hip/hip_bf16.h>
#include <stdint.h>

#define K_XCDMAP 1
#define K_G0 2
#define K_LEAD 50
#define K_LDSPAD 1

typedef __attribute__((ext_vector_type(4))) unsigned u32x4;
typedef __attribute__((ext_vector_type(4))) float f32x4;
typedef __attribute__((ext_vector_type(2))) float f32x2;
typedef __attribute__((ext_vector_type(8))) __bf16 bf16x8;
typedef __attribute__((ext_vector_type(2))) __bf16 bf16x2;
typedef __amdgpu_buffer_rsrc_t rsrc_t;

constexpr int NB = 4, HKV = 16, SK = 4096, D = 128;
constexpr int NGRP = NB * HKV;
constexpr int NSPLIT = 4, WAVES = 4, NTHR = WAVES * 64;
constexpr int KPS = SK / NSPLIT;
constexpr int NLW = KPS / 4 / WAVES;
constexpr int WIN = K_WIN;
constexpr int NIT = NLW / WIN, NT = WIN / 4;
constexpr float C2 = 0.08838834764831845f * 1.4426950408889634f;
constexpr int ITB = WIN * WAVES * 1024;
constexpr int NG = 2 * NLW / 4;
constexpr double STEPS_P = NG - K_G0, STEPS_C = K_GB - K_G0 + 1 < STEPS_P ? K_GB - K_G0 + 1 : STEPS_P;
constexpr double TAU_P = K_RATE ? (NGRP * (NSPLIT - 1) + NGRP * (STEPS_C / (STEPS_P + (K_GB < NG ? 1 : 0)))) * 16384.0 / (K_RATE * 1e9) * 1e8 * 256 : 0;
constexpr double TAU_C = TAU_P * (STEPS_P + (K_GB < NG ? 1 : 0)) / STEPS_C;

constexpr size_t WS_EPOCH = 0;
constexpr size_t WS_PART = 64 * 1024;

#define STAMP_OFF (8u << 20)
#define STAMP_NW (NGRP * NSPLIT * WAVES)
#define STAMP_N 7
#define STAMP(i) do { __builtin_amdgcn_sched_barrier(0); unsigned long long t_ = __builtin_amdgcn_s_memrealtime(); stampv[i] = t_; __builtin_amdgcn_sched_barrier(0); } while (0)
#define STAMP_FLUSH() do { if (lane == 0) { unsigned long long* sp_ = (unsigned long long*)(ws + STAMP_OFF) + ((size_t)blockIdx.x * WAVES + wave) * 8; for (int i_ = 0; i_ < STAMP_N; ++i_) sp_[i_] = stampv[i_]; } } while (0)

__device__ __forceinline__ float dot2(unsigned a, unsigned b, float acc) {
  return __builtin_amdgcn_fdot2_f32_bf16(__builtin_bit_cast(bf16x2, a), __builtin_bit_cast(bf16x2, b), acc, false);
}
template <int CTRL> __device__ __forceinline__ float dpp(float x) {
  return __builtin_bit_cast(float, __builtin_amdgcn_mov_dpp(__builtin_bit_cast(int, x), CTRL, 0xf, 0xf, true));
}
constexpr int XOR1 = 0xB1, XOR2 = 0x4E, ROR4 = 0x124, ROR8 = 0x128, ROR12 = 0x12C, IDENT = 0xE4;
__device__ __forceinline__ float xrow_max(float x) {
  auto s = __builtin_amdgcn_permlane16_swap(__float_as_uint(x), __float_as_uint(x), false, false);
  x = fmaxf(__uint_as_float(s[0]), __uint_as_float(s[1]));
  auto t = __builtin_amdgcn_permlane32_swap(__float_as_uint(x), __float_as_uint(x), false, false);
  return fmaxf(__uint_as_float(t[0]), __uint_as_float(t[1]));
}
__device__ __forceinline__ float xrow_sum(float x) {
  auto s = __builtin_amdgcn_permlane16_swap(__float_as_uint(x), __float_as_uint(x), false, false);
  x = __uint_as_float(s[0]) + __uint_as_float(s[1]);
  auto t = __builtin_amdgcn_permlane32_swap(__float_as_uint(x), __float_as_uint(x), false, false);
  return __uint_as_float(t[0]) + __uint_as_float(t[1]);
}
__device__ __forceinline__ rsrc_t mkrsrc(const unsigned char* p) {
  return __builtin_amdgcn_make_buffer_rsrc((void*)p, 0, ITB, 0x00020000);
}
__device__ __forceinline__ u32x4 ldstream(rsrc_t r, int voff, int u) {
  u32x4 v = __builtin_amdgcn_raw_buffer_load_b128(r, voff, u * 4096, 2);
  asm volatile("" ::: "memory");
  __builtin_amdgcn_sched_barrier(0);
  return v;
}
__device__ __forceinline__ void pace(unsigned& tgt, unsigned tau, unsigned tend) {
  asm volatile("s_cmp_eq_u32 %0, %2\n\t"
               "s_cbranch_scc1 2f\n\t"
               "s_add_u32 %0, %0, %1\n\t"
               "s_branch 1f\n"
               "0:\n\t"
               "s_sleep 1\n"
               "1:\n\t"
               "s_memrealtime s[96:97]\n\t"
               "s_waitcnt lgkmcnt(0)\n\t"
               "s_lshl_b32 s96, s96, 8\n\t"
               "s_sub_u32 s96, s96, %0\n\t"
               "s_cmp_lt_i32 s96, 0\n\t"
               "s_cbranch_scc1 0b\n"
               "2:" : "+s"(tgt) : "s"(tau), "s"(tend) : "s96", "s97", "scc", "memory");
  __builtin_amdgcn_sched_barrier(0);
}
template <int R, int B> __device__ __forceinline__ unsigned place(unsigned old, unsigned src) {
  constexpr int n = (R - B) & 3;
  constexpr int ctrl = n == 0 ? IDENT : n == 1 ? ROR4 : n == 2 ? ROR8 : ROR12;
  return (unsigned)__builtin_amdgcn_update_dpp((int)old, (int)src, ctrl, 0xf, 1 << R, false);
}
template <int B> __device__ __forceinline__ void place4(u32x4& a, unsigned src) {
  a[0] = place<0, B>(a[0], src); a[1] = place<1, B>(a[1], src); a[2] = place<2, B>(a[2], src); a[3] = place<3, B>(a[3], src);
}
__device__ __forceinline__ f32x4 mfma(u32x4 a, u32x4 b, f32x4 c) {
  c = __builtin_amdgcn_mfma_f32_16x16x32_bf16(__builtin_bit_cast(bf16x8, a), __builtin_bit_cast(bf16x8, b), c, 0, 0, 0);
  asm volatile("" : "+a"(c));
  return c;
}

constexpr int LDS_T = 0;
constexpr int LDS_O = WAVES * (NLW / 4) * 64;
constexpr int LDS_M = LDS_O + WAVES * 256;
constexpr int LDS_L = LDS_M + 8;
constexpr int LDS_N = K_LDSPAD ? 21 * 1024 : LDS_L + 8;

__global__ void __launch_bounds__(NTHR)
gqa_decode_kernel(const u32x4* __restrict__ q, const unsigned char* kc, const unsigned char* vc,
                  unsigned short* __restrict__ out, unsigned char* __restrict__ ws) {
  __shared__ __attribute__((aligned(16))) float lds[LDS_N];
  unsigned long long stampv[STAMP_N] = {};
  STAMP(0);
  const unsigned tbase = (unsigned)__builtin_amdgcn_s_memrealtime() + K_LEAD;
  const int tid = threadIdx.x, lane = tid & 63;
  const int wave = __builtin_amdgcn_readfirstlane(tid >> 6);
  const int lin = (blockIdx.x & 7) * (NGRP * NSPLIT / 8) + (blockIdx.x >> 3);
  const int grp = lin / NSPLIT, split = lin % NSPLIT;
  const int c = lane & 15, hl = (lane >> 1) & 1;

  const u32x4 qa = q[grp * 32 + hl * 16 + c];
  const u32x4 qb = q[grp * 32 + (hl ^ 1) * 16 + c];
  unsigned* ep = reinterpret_cast<unsigned*>(ws + WS_EPOCH + ((size_t)grp * 4 + wave) * 128);
  const unsigned epoch = __hip_atomic_load(ep, __ATOMIC_RELAXED, __HIP_MEMORY_SCOPE_AGENT);

  const size_t base = (size_t)lin * (KPS * D * 2);
  const unsigned char* kp = kc + base;
  const unsigned char* vp = vc + base;
  const int voff = lane * 16 + wave * 1024;

  const unsigned tau = split ? (unsigned)TAU_P : (unsigned)TAU_C;
  unsigned tgt = tbase << 8;
  const unsigned tend = tgt + (split ? (unsigned)TAU_P * (unsigned)STEPS_P : (unsigned)TAU_C * (unsigned)STEPS_C);
  u32x4 ring[WIN];
  {
    const rsrc_t r = mkrsrc(kp);
#pragma unroll
    for (int u = 0; u < WIN; ++u) {
      if (u % 4 == 0 && u / 4 >= K_G0) pace(tgt, tau, tend);
      ring[u] = ldstream(r, voff, u);
    }
  }
  float* sT = lds + LDS_T + wave * (NLW / 4) * 64;
  float mx = -INFINITY;
  const bool b3 = lane & 8, b2 = lane & 4;
#pragma unroll 1
  for (int it = 0; it < NIT; ++it) {
    const rsrc_t r = mkrsrc(it + 1 < NIT ? kp + (size_t)(it + 1) * ITB : vp);
#pragma unroll
    for (int a = 0; a < NT; ++a) {
      float z[4];
#pragma unroll
      for (int b = 0; b < 4; ++b) {
        const int u = a * 4 + b;
        float da = 0.f, db = 0.f;
#pragma unroll
        for (int k = 0; k < 4; ++k) { da = dot2(ring[u][k], qa[k], da); db = dot2(ring[u][k], qb[k], db); }
        asm volatile("" : "+v"(da), "+v"(db));
        __builtin_amdgcn_sched_barrier(0);
        if (b == 0) pace(tgt, tau, tend);
        ring[u] = ldstream(r, voff, u);
        z[b] = da + dpp<XOR2>(db);
      }
      const float u0 = z[0] + dpp<ROR8>(z[0]), u2 = z[2] + dpp<ROR8>(z[2]);
      const float u1 = z[1] + dpp<ROR8>(z[1]), u3 = z[3] + dpp<ROR8>(z[3]);
      const float w0 = b3 ? u2 : u0, w1 = b3 ? u3 : u1;
      const float x0 = w0 + dpp<0x104>(w0);
      const float x1 = w1 + dpp<0x114>(w1);
      float t = b2 ? x1 : x0;
      t += dpp<XOR1>(t);
      mx = fmaxf(mx, t);
      sT[(it * NT + a) * 64 + lane] = t;
      __builtin_amdgcn_sched_barrier(0);
    }
  }
  STAMP(1);
  mx = fmaxf(mx, dpp<ROR4>(mx)); mx = fmaxf(mx, dpp<ROR8>(mx)); mx = xrow_max(mx);
  const float mc = mx * C2;
  const unsigned hm = (lane & 1) ? 0xffff0000u : 0x0000ffffu;
  float l = 0.f;
  u32x4* part = reinterpret_cast<u32x4*>(ws + WS_PART) + (size_t)grp * NSPLIT * 256 + tid;
  u32x4 r[NSPLIT - 1] = {};

  f32x4 acc = {0.f, 0.f, 0.f, 0.f};
  u32x4 A0 = {0u, 0u, 0u, 0u}, A1 = {0u, 0u, 0u, 0u};
#pragma unroll 1
  for (int it = 0; it < NIT; ++it) {
    const rsrc_t rv = __builtin_amdgcn_make_buffer_rsrc((void*)(vp + (size_t)((it + 1) & (NIT - 1)) * ITB), 0, it + 1 < NIT ? ITB : 0, 0x00020000);
    const rsrc_t rp = __builtin_amdgcn_make_buffer_rsrc((void*)(ws + WS_PART + (size_t)grp * NSPLIT * 4096), 0, it + 1 == NIT && split == 0 ? NSPLIT * 4096 : 0, 0x00020000);
    unsigned P[NT];
#pragma unroll
    for (int a = 0; a < NT; ++a) {
      const float p = __builtin_amdgcn_exp2f(fmaf(sT[(it * NT + a) * 64 + lane], C2, -mc));
      l += p;
      bf16x2 b; b[0] = (__bf16)p; b[1] = b[0];
      P[a] = __builtin_bit_cast(unsigned, b) & hm;
    }
#pragma unroll
    for (int a = 0; a < NT; ++a) {
      place4<0>(A0, P[a]); acc = mfma(A0, ring[a * 4 + 0], acc); __builtin_amdgcn_sched_barrier(0);
      pace(tgt, tau, tend);
      if (a == K_POLLA % NT) {
#pragma unroll
        for (int s = 1; s < NSPLIT; ++s) {
          asm volatile("" :: "v"(r[s - 1]));
          r[s - 1] = __builtin_amdgcn_raw_buffer_load_b128(rp, voff, s * 4096, 16);
        }
        asm volatile("" ::: "memory");
        __builtin_amdgcn_sched_barrier(0);
      }
      ring[a * 4 + 0] = ldstream(rv, voff, a * 4 + 0);
      place4<1>(A1, P[a]); acc = mfma(A1, ring[a * 4 + 1], acc); __builtin_amdgcn_sched_barrier(0);
      ring[a * 4 + 1] = ldstream(rv, voff, a * 4 + 1);
      place4<2>(A0, P[a]); acc = mfma(A0, ring[a * 4 + 2], acc); __builtin_amdgcn_sched_barrier(0);
      ring[a * 4 + 2] = ldstream(rv, voff, a * 4 + 2);
      place4<3>(A1, P[a]); acc = mfma(A1, ring[a * 4 + 3], acc); __builtin_amdgcn_sched_barrier(0);
      ring[a * 4 + 3] = ldstream(rv, voff, a * 4 + 3);
    }
  }
  {
    float* so = lds + LDS_O + wave * 256 + c * 8 + (lane >> 4) * 2;
    *reinterpret_cast<f32x2*>(so) = f32x2{acc[0], acc[1]};
    *reinterpret_cast<f32x2*>(so + 128) = f32x2{acc[2], acc[3]};
    l += dpp<ROR4>(l); l += dpp<ROR8>(l); l = xrow_sum(l);
    if ((lane & 61) == 0) { lds[LDS_M + hl * 4 + wave] = mc; lds[LDS_L + hl * 4 + wave] = l; }
  }
  STAMP(2);
  __syncthreads();

  const f32x4 tl = *reinterpret_cast<const f32x4*>(lds + LDS_L + (tid >> 7) * 4);
  const f32x4 tm = *reinterpret_cast<const f32x4*>(lds + LDS_M + (tid >> 7) * 4);
  const float M = fmaxf(fmaxf(tm[0], tm[1]), fmaxf(tm[2], tm[3]));
  float o = 0.f, L = 0.f;
#pragma unroll
  for (int w = 0; w < WAVES; ++w) {
    const float f = __builtin_amdgcn_exp2f(tm[w] - M);
    o = fmaf(f, lds[LDS_O + w * 256 + tid], o); L = fmaf(f, tl[w], L);
  }
  const float on = o * __builtin_amdgcn_rcpf(L);
  const float lse = M + __builtin_amdgcn_logf(L);
  const unsigned tag = epoch + 0x9E3779B9u;
  if (split != 0) {
    u32x4 rec = {tag, __float_as_uint(on), tag, __float_as_uint(lse)};
    asm volatile("global_store_dwordx4 %0, %1, off sc1\n\ts_nop 1" :: "v"(part + split * 256), "v"(rec) : "memory");
    STAMP(3); STAMP_FLUSH();
    return;
  }
  STAMP(3);
  const rsrc_t rp = __builtin_amdgcn_make_buffer_rsrc((void*)(ws + WS_PART + (size_t)grp * NSPLIT * 4096), 0, NSPLIT * 4096, 0x00020000);
#pragma unroll 1
  for (unsigned spin = 0; spin < (1u << 20); ++spin) {
    bool ok = true;
#pragma unroll
    for (int s = 1; s < NSPLIT; ++s) ok &= (r[s - 1][0] == tag) & (r[s - 1][2] == tag);
    if (__all(ok)) break;
#pragma unroll
    for (int s = 1; s < NSPLIT; ++s) r[s - 1] = __builtin_amdgcn_raw_buffer_load_b128(rp, voff, s * 4096, 16);
    asm volatile("" ::: "memory");
  }
  float Mx = lse;
#pragma unroll
  for (int s = 1; s < NSPLIT; ++s) Mx = fmaxf(Mx, __uint_as_float(r[s - 1][3]));
  float wsum = __builtin_amdgcn_exp2f(lse - Mx), osum = wsum * on;
#pragma unroll
  for (int s = 1; s < NSPLIT; ++s) {
    const float w = __builtin_amdgcn_exp2f(__uint_as_float(r[s - 1][3]) - Mx);
    wsum += w; osum = fmaf(w, __uint_as_float(r[s - 1][1]), osum);
  }
  out[grp * 256 + tid] = __builtin_bit_cast(unsigned short, (__bf16)(osum * __builtin_amdgcn_rcpf(wsum)));
  if (lane == 0) __hip_atomic_store(ep, tag, __ATOMIC_RELAXED, __HIP_MEMORY_SCOPE_AGENT);
  STAMP(4); STAMP_FLUSH();
}

#define K_REF 10
__device__ unsigned stat_one(const unsigned long long* sp, unsigned ref, int code, unsigned* sh) {
  const int ph = code % 10, kind = code / 10;
  unsigned mx = 0, mn = ~0u, sm = 0, n = 0;
  for (int i = threadIdx.x; i < STAMP_NW; i += 256) {
    unsigned long long braw = sp[8 * i + ph]; unsigned b = (unsigned)braw - ref;
    if (braw) { mx = max(mx, b); mn = min(mn, b); sm += b; ++n; }
  }
  for (int o = 32; o; o >>= 1) { mx = max(mx, (unsigned)__shfl_xor(mx, o)); mn = min(mn, (unsigned)__shfl_xor(mn, o)); sm += __shfl_xor(sm, o); n += __shfl_xor(n, o); }
  __syncthreads();
  if ((threadIdx.x & 63) == 0) { int w = threadIdx.x >> 6; sh[w] = mx; sh[4 + w] = mn; sh[8 + w] = sm; sh[12 + w] = n; }
  __syncthreads();
  mx = max(max(sh[0], sh[1]), max(sh[2], sh[3])); mn = min(min(sh[4], sh[5]), min(sh[6], sh[7]));
  sm = sh[8] + sh[9] + sh[10] + sh[11]; n = sh[12] + sh[13] + sh[14] + sh[15];
  return kind == 0 ? mx : kind == 1 ? mn : sm / n;
}
__global__ void gqa_stat_kernel(unsigned char* ws) {
  const unsigned long long* sp = (const unsigned long long*)(ws + STAMP_OFF);
  const unsigned ref = (unsigned)sp[0] - 100000u;
  __shared__ unsigned sh[16];
  const unsigned a = stat_one(sp, ref, K_STAT < 0 ? 0 : K_STAT, sh), b = stat_one(sp, ref, K_REF, sh);
  unsigned dl = K_STAT < 0 ? ((a - b) >> 20) : K_SCALE * (a - b);
  if (dl > 100000) dl = 100000;
  const unsigned long long t0 = __builtin_amdgcn_s_memrealtime();
  while (__builtin_amdgcn_s_memrealtime() - t0 < dl) __builtin_amdgcn_s_sleep(2);
}
extern "C" void kernel_launch(void* const* d_in, const int* in_sizes, int n_in,
                              void* d_out, int out_size, void* d_ws, size_t ws_size,
                              hipStream_t stream) {
  (void)in_sizes; (void)n_in; (void)out_size; (void)ws_size;
  gqa_decode_kernel<<<NGRP * NSPLIT, NTHR, 0, stream>>>((const u32x4*)d_in[0], (const unsigned char*)d_in[1], (const unsigned char*)d_in[2],
                                                        (unsigned short*)d_out, (unsigned char*)d_ws);
  gqa_stat_kernel<<<1, 256, 0, stream>>>((unsigned char*)d_ws);
}
```

```cpp
#define K_WIN 16
#define K_GB 32
#define K_POLLA 3
#define K_RATE 7600
#define K_STAMP 1
#define K_STAT -1
#define K_SCALE 1
#include <hip/hip_runtime.h>
#include <hip/hip_bf16.h>
#include <stdint.h>

#define K_XCDMAP 1
#define K_G0 2
#define K_LEAD 50
#define K_LDSPAD 1

typedef __attribute__((ext_vector_type(4))) unsigned u32x4;
typedef __attribute__((ext_vector_type(4))) float f32x4;
typedef __attribute__((ext_vector_type(2))) float f32x2;
typedef __attribute__((ext_vector_type(8))) __bf16 bf16x8;
typedef __attribute__((ext_vector_type(2))) __bf16 bf16x2;
typedef __amdgpu_buffer_rsrc_t rsrc_t;

constexpr int NB = 4, HKV = 16, SK = 4096, D = 128;
constexpr int NGRP = NB * HKV;
constexpr int NSPLIT = 4, WAVES = 4, NTHR = WAVES * 64;
constexpr int KPS = SK / NSPLIT;
constexpr int NLW = KPS / 4 / WAVES;
constexpr int WIN = K_WIN;
constexpr int NIT = NLW / WIN, NT = WIN / 4;
constexpr float C2 = 0.08838834764831845f * 1.4426950408889634f;
constexpr int ITB = WIN * WAVES * 1024;
constexpr int NG = 2 * NLW / 4;
constexpr double STEPS_P = NG - K_G0, STEPS_C = K_GB - K_G0 + 1 < STEPS_P ? K_GB - K_G0 + 1 : STEPS_P;
constexpr double TAU_P = K_RATE ? (NGRP * (NSPLIT - 1) + NGRP * (STEPS_C / (STEPS_P + (K_GB < NG ? 1 : 0)))) * 16384.0 / (K_RATE * 1e9) * 1e8 * 256 : 0;
constexpr double TAU_C = TAU_P * (STEPS_P + (K_GB < NG ? 1 : 0)) / STEPS_C;

constexpr size_t WS_EPOCH = 0;
constexpr size_t WS_PART = 64 * 1024;

#define STAMP_OFF (8u << 20)
#define STAMP_NW (NGRP * NSPLIT * WAVES)
#define STAMP_N 7
#define STAMP(i) do { __builtin_amdgcn_sched_barrier(0); unsigned long long t_ = __builtin_amdgcn_s_memrealtime(); stampv[i] = t_; __builtin_amdgcn_sched_barrier(0); } while (0)
#define STAMP_FLUSH() do { if (lane == 0) { unsigned long long* sp_ = (unsigned long long*)(ws + STAMP_OFF) + ((size_t)blockIdx.x * WAVES + wave) * 8; for (int i_ = 0; i_ < STAMP_N; ++i_) sp_[i_] = stampv[i_]; } } while (0)

__device__ __forceinline__ float dot2(unsigned a, unsigned b, float acc) {
  return __builtin_amdgcn_fdot2_f32_bf16(__builtin_bit_cast(bf16x2, a), __builtin_bit_cast(bf16x2, b), acc, false);
}
template <int CTRL> __device__ __forceinline__ float dpp(float x) {
  return __builtin_bit_cast(float, __builtin_amdgcn_mov_dpp(__builtin_bit_cast(int, x), CTRL, 0xf, 0xf, true));
}
constexpr int XOR1 = 0xB1, XOR2 = 0x4E, ROR4 = 0x124, ROR8 = 0x128, ROR12 = 0x12C, IDENT = 0xE4;
__device__ __forceinline__ float xrow_max(float x) {
  auto s = __builtin_amdgcn_permlane16_swap(__float_as_uint(x), __float_as_uint(x), false, false);
  x = fmaxf(__uint_as_float(s[0]), __uint_as_float(s[1]));
  auto t = __builtin_amdgcn_permlane32_swap(__float_as_uint(x), __float_as_uint(x), false, false);
  return fmaxf(__uint_as_float(t[0]), __uint_as_float(t[1]));
}
__device__ __forceinline__ float xrow_sum(float x) {
  auto s = __builtin_amdgcn_permlane16_swap(__float_as_uint(x), __float_as_uint(x), false, false);
  x = __uint_as_float(s[0]) + __uint_as_float(s[1]);
  auto t = __builtin_amdgcn_permlane32_swap(__float_as_uint(x), __float_as_uint(x), false, false);
  return __uint_as_float(t[0]) + __uint_as_float(t[1]);
}
__device__ __forceinline__ rsrc_t mkrsrc(const unsigned char* p) {
  return __builtin_amdgcn_make_buffer_rsrc((void*)p, 0, ITB, 0x00020000);
}
__device__ __forceinline__ u32x4 ldstream(rsrc_t r, int voff, int u) {
  u32x4 v = __builtin_amdgcn_raw_buffer_load_b128(r, voff, u * 4096, 2);
  asm volatile("" ::: "memory");
  __builtin_amdgcn_sched_barrier(0);
  return v;
}
__device__ __forceinline__ void pace(unsigned& tgt, unsigned tau, unsigned tend) {
  asm volatile("s_cmp_eq_u32 %0, %2\n\t"
               "s_cbranch_scc1 2f\n\t"
               "s_add_u32 %0, %0, %1\n\t"
               "s_branch 1f\n"
               "0:\n\t"
               "s_sleep 1\n"
               "1:\n\t"
               "s_memrealtime s[96:97]\n\t"
               "s_waitcnt lgkmcnt(0)\n\t"
               "s_lshl_b32 s96, s96, 8\n\t"
               "s_sub_u32 s96, s96, %0\n\t"
               "s_cmp_lt_i32 s96, 0\n\t"
               "s_cbranch_scc1 0b\n"
               "2:" : "+s"(tgt) : "s"(tau), "s"(tend) : "s96", "s97", "scc", "memory");
  __builtin_amdgcn_sched_barrier(0);
}
template <int R, int B> __device__ __forceinline__ unsigned place(unsigned old, unsigned src) {
  constexpr int n = (R - B) & 3;
  constexpr int ctrl = n == 0 ? IDENT : n == 1 ? ROR4 : n == 2 ? ROR8 : ROR12;
  return (unsigned)__builtin_amdgcn_update_dpp((int)old, (int)src, ctrl, 0xf, 1 << R, false);
}
template <int B> __device__ __forceinline__ void place4(u32x4& a, unsigned src) {
  a[0] = place<0, B>(a[0], src); a[1] = place<1, B>(a[1], src); a[2] = place<2, B>(a[2], src); a[3] = place<3, B>(a[3], src);
}
__device__ __forceinline__ f32x4 mfma(u32x4 a, u32x4 b, f32x4 c) {
  c = __builtin_amdgcn_mfma_f32_16x16x32_bf16(__builtin_bit_cast(bf16x8, a), __builtin_bit_cast(bf16x8, b), c, 0, 0, 0);
  asm volatile("" : "+a"(c));
  return c;
}

constexpr int LDS_T = 0;
constexpr int LDS_O = WAVES * (NLW / 4) * 64;
constexpr int LDS_M = LDS_O + WAVES * 256;
constexpr int LDS_L = LDS_M + 8;
constexpr int LDS_N = K_LDSPAD ? 21 * 1024 : LDS_L + 8;

__global__ void __launch_bounds__(NTHR)
gqa_decode_kernel(const u32x4* __restrict__ q, const unsigned char* kc, const unsigned char* vc,
                  unsigned short* __restrict__ out, unsigned char* __restrict__ ws) {
  __shared__ __attribute__((aligned(16))) float lds[LDS_N];
  unsigned long long stampv[STAMP_N] = {};
  STAMP(0);
  const unsigned tbase = (unsigned)__builtin_amdgcn_s_memrealtime() + K_LEAD;
  const int tid = threadIdx.x, lane = tid & 63;
  const int wave = __builtin_amdgcn_readfirstlane(tid >> 6);
  const int lin = (blockIdx.x & 7) * (NGRP * NSPLIT / 8) + (blockIdx.x >> 3);
  const int grp = lin / NSPLIT, split = lin % NSPLIT;
  const int c = lane & 15, hl = (lane >> 1) & 1;

  const u32x4 qa = q[grp * 32 + hl * 16 + c];
  const u32x4 qb = q[grp * 32 + (hl ^ 1) * 16 + c];
  unsigned* ep = reinterpret_cast<unsigned*>(ws + WS_EPOCH + ((size_t)grp * 4 + wave) * 128);
  const unsigned epoch = __hip_atomic_load(ep, __ATOMIC_RELAXED, __HIP_MEMORY_SCOPE_AGENT);

  const size_t base = (size_t)lin * (KPS * D * 2);
  const unsigned char* kp = kc + base;
  const unsigned char* vp = vc + base;
  const int voff = lane * 16 + wave * 1024;

  const unsigned tau = split ? (unsigned)TAU_P : (unsigned)TAU_C;
  unsigned tgt = tbase << 8;
  const unsigned tend = tgt + (split ? (unsigned)TAU_P * (unsigned)STEPS_P : (unsigned)TAU_C * (unsigned)STEPS_C);
  u32x4 ring[WIN];
  {
    const rsrc_t r = mkrsrc(kp);
#pragma unroll
    for (int u = 0; u < WIN; ++u) {
      if (u % 4 == 0 && u / 4 >= K_G0) pace(tgt, tau, tend);
      ring[u] = ldstream(r, voff, u);
    }
  }
  float* sT = lds + LDS_T + wave * (NLW / 4) * 64;
  float mx = -INFINITY;
  const bool b3 = lane & 8, b2 = lane & 4;
#pragma unroll 1
  for (int it = 0; it < NIT; ++it) {
    const rsrc_t r = mkrsrc(it + 1 < NIT ? kp + (size_t)(it + 1) * ITB : vp);
#pragma unroll
    for (int a = 0; a < NT; ++a) {
      float z[4];
#pragma unroll
      for (int b = 0; b < 4; ++b) {
        const int u = a * 4 + b;
        float da = 0.f, db = 0.f;
#pragma unroll
        for (int k = 0; k < 4; ++k) { da = dot2(ring[u][k], qa[k], da); db = dot2(ring[u][k], qb[k], db); }
        asm volatile("" : "+v"(da), "+v"(db));
        __builtin_amdgcn_sched_barrier(0);
        if (b == 0) pace(tgt, tau, tend);
        ring[u] = ldstream(r, voff, u);
        z[b] = da + dpp<XOR2>(db);
      }
      const float u0 = z[0] + dpp<ROR8>(z[0]), u2 = z[2] + dpp<ROR8>(z[2]);
      const float u1 = z[1] + dpp<ROR8>(z[1]), u3 = z[3] + dpp<ROR8>(z[3]);
      const float w0 = b3 ? u2 : u0, w1 = b3 ? u3 : u1;
      const float x0 = w0 + dpp<0x104>(w0);
      const float x1 = w1 + dpp<0x114>(w1);
      float t = b2 ? x1 : x0;
      t += dpp<XOR1>(t);
      mx = fmaxf(mx, t);
      sT[(it * NT + a) * 64 + lane] = t;
      __builtin_amdgcn_sched_barrier(0);
    }
  }
  STAMP(1);
  mx = fmaxf(mx, dpp<ROR4>(mx)); mx = fmaxf(mx, dpp<ROR8>(mx)); mx = xrow_max(mx);
  const float mc = mx * C2;
  const unsigned hm = (lane & 1) ? 0xffff0000u : 0x0000ffffu;
  float l = 0.f;
  u32x4* part = reinterpret_cast<u32x4*>(ws + WS_PART) + (size_t)grp * NSPLIT * 256 + tid;
  u32x4 r[NSPLIT - 1] = {};

  f32x4 acc = {0.f, 0.f, 0.f, 0.f};
  u32x4 A0 = {0u, 0u, 0u, 0u}, A1 = {0u, 0u, 0u, 0u};
#pragma unroll 1
  for (int it = 0; it < NIT; ++it) {
    const rsrc_t rv = __builtin_amdgcn_make_buffer_rsrc((void*)(vp + (size_t)((it + 1) & (NIT - 1)) * ITB), 0, it + 1 < NIT ? ITB : 0, 0x00020000);
    const rsrc_t rp = __builtin_amdgcn_make_buffer_rsrc((void*)(ws + WS_PART + (size_t)grp * NSPLIT * 4096), 0, it + 1 == NIT && split == 0 ? NSPLIT * 4096 : 0, 0x00020000);
    unsigned P[NT];
#pragma unroll
    for (int a = 0; a < NT; ++a) {
      const float p = __builtin_amdgcn_exp2f(fmaf(sT[(it * NT + a) * 64 + lane], C2, -mc));
      l += p;
      bf16x2 b; b[0] = (__bf16)p; b[1] = b[0];
      P[a] = __builtin_bit_cast(unsigned, b) & hm;
    }
#pragma unroll
    for (int a = 0; a < NT; ++a) {
      place4<0>(A0, P[a]); acc = mfma(A0, ring[a * 4 + 0], acc); __builtin_amdgcn_sched_barrier(0);
      pace(tgt, tau, tend);
      if (a == K_POLLA % NT) {
#pragma unroll
        for (int s = 1; s < NSPLIT; ++s) {
          asm volatile("" :: "v"(r[s - 1]));
          r[s - 1] = __builtin_amdgcn_raw_buffer_load_b128(rp, voff, s * 4096, 16);
        }
        asm volatile("" ::: "memory");
        __builtin_amdgcn_sched_barrier(0);
      }
      ring[a * 4 + 0] = ldstream(rv, voff, a * 4 + 0);
      place4<1>(A1, P[a]); acc = mfma(A1, ring[a * 4 + 1], acc); __builtin_amdgcn_sched_barrier(0);
      ring[a * 4 + 1] = ldstream(rv, voff, a * 4 + 1);
      place4<2>(A0, P[a]); acc = mfma(A0, ring[a * 4 + 2], acc); __builtin_amdgcn_sched_barrier(0);
      ring[a * 4 + 2] = ldstream(rv, voff, a * 4 + 2);
      place4<3>(A1, P[a]); acc = mfma(A1, ring[a * 4 + 3], acc); __builtin_amdgcn_sched_barrier(0);
      ring[a * 4 + 3] = ldstream(rv, voff, a * 4 + 3);
    }
  }
  {
    float* so = lds + LDS_O + wave * 256 + c * 8 + (lane >> 4) * 2;
    *reinterpret_cast<f32x2*>(so) = f32x2{acc[0], acc[1]};
    *reinterpret_cast<f32x2*>(so + 128) = f32x2{acc[2], acc[3]};
    l += dpp<ROR4>(l); l += dpp<ROR8>(l); l = xrow_sum(l);
    if ((lane & 61) == 0) { lds[LDS_M + hl * 4 + wave] = mc; lds[LDS_L + hl * 4 + wave] = l; }
  }
  STAMP(2);
  __syncthreads();

  const f32x4 tl = *reinterpret_cast<const f32x4*>(lds + LDS_L + (tid >> 7) * 4);
  const f32x4 tm = *reinterpret_cast<const f32x4*>(lds + LDS_M + (tid >> 7) * 4);
  const float M = fmaxf(fmaxf(tm[0], tm[1]), fmaxf(tm[2], tm[3]));
  float o = 0.f, L = 0.f;
#pragma unroll
  for (int w = 0; w < WAVES; ++w) {
    const float f = __builtin_amdgcn_exp2f(tm[w] - M);
    o = fmaf(f, lds[LDS_O + w * 256 + tid], o); L = fmaf(f, tl[w], L);
  }
  const float on = o * __builtin_amdgcn_rcpf(L);
  const float lse = M + __builtin_amdgcn_logf(L);
  const unsigned tag = epoch + 0x9E3779B9u;
  if (split != 0) {
    u32x4 rec = {tag, __float_as_uint(on), tag, __float_as_uint(lse)};
    asm volatile("global_store_dwordx4 %0, %1, off sc1\n\ts_nop 1" :: "v"(part + split * 256), "v"(rec) : "memory");
    STAMP(3); STAMP_FLUSH();
    return;
  }
  STAMP(3);
  const rsrc_t rp = __builtin_amdgcn_make_buffer_rsrc((void*)(ws + WS_PART + (size_t)grp * NSPLIT * 4096), 0, NSPLIT * 4096, 0x00020000);
#pragma unroll 1
  for (unsigned spin = 0; spin < (1u << 20); ++spin) {
    bool ok = true;
#pragma unroll
    for (int s = 1; s < NSPLIT; ++s) ok &= (r[s - 1][0] == tag) & (r[s - 1][2] == tag);
    if (__all(ok)) break;
#pragma unroll
    for (int s = 1; s < NSPLIT; ++s) r[s - 1] = __builtin_amdgcn_raw_buffer_load_b128(rp, voff, s * 4096, 16);
    asm volatile("" ::: "memory");
  }
  float Mx = lse;
#pragma unroll
  for (int s = 1; s < NSPLIT; ++s) Mx = fmaxf(Mx, __uint_as_float(r[s - 1][3]));
  float wsum = __builtin_amdgcn_exp2f(lse - Mx), osum = wsum * on;
#pragma unroll
  for (int s = 1; s < NSPLIT; ++s) {
    const float w = __builtin_amdgcn_exp2f(__uint_as_float(r[s - 1][3]) - Mx);
    wsum += w; osum = fmaf(w, __uint_as_float(r[s - 1][1]), osum);
  }
  out[grp * 256 + tid] = __builtin_bit_cast(unsigned short, (__bf16)(osum * __builtin_amdgcn_rcpf(wsum)));
  if (lane == 0) __hip_atomic_store(ep, tag, __ATOMIC_RELAXED, __HIP_MEMORY_SCOPE_AGENT);
  STAMP(4); STAMP_FLUSH();
}

#define K_REF 10
__device__ unsigned stat_one(const unsigned long long* sp, unsigned ref, int code, unsigned* sh) {
  const int ph = code % 10, kind = code / 10;
  unsigned mx = 0, mn = ~0u, sm = 0, n = 0;
  for (int i = threadIdx.x; i < STAMP_NW; i += 256) {
    unsigned long long braw = sp[8 * i + ph]; unsigned b = (unsigned)braw - ref;
    if (braw) { mx = max(mx, b); mn = min(mn, b); sm += b; ++n; }
  }
  if (kind == 3 || kind == 4) {
    unsigned s2 = sm, n2 = n;
    s2 += __shfl_xor(s2, 1); n2 += __shfl_xor(n2, 1); s2 += __shfl_xor(s2, 2); n2 += __shfl_xor(n2, 2); s2 += __shfl_xor(s2, 32); n2 += __shfl_xor(n2, 32);
    __shared__ unsigned cs[64];
    __syncthreads();
    if ((threadIdx.x & 35) == 0) { int c = (threadIdx.x >> 2) & 7, w = threadIdx.x >> 6; cs[w * 8 + c] = s2; cs[32 + w * 8 + c] = n2; }
    __syncthreads();
    unsigned best = kind == 3 ? 0u : ~0u;
    for (int c = 0; c < 8; ++c) { unsigned m = (cs[c] + cs[8 + c] + cs[16 + c] + cs[24 + c]) / (cs[32 + c] + cs[40 + c] + cs[48 + c] + cs[56 + c]); best = kind == 3 ? max(best, m) : min(best, m); }
    return best;
  }
  for (int o = 32; o; o >>= 1) { mx = max(mx, (unsigned)__shfl_xor(mx, o)); mn = min(mn, (unsigned)__shfl_xor(mn, o)); sm += __shfl_xor(sm, o); n += __shfl_xor(n, o); }
  __syncthreads();
  if ((threadIdx.x & 63) == 0) { int w = threadIdx.x >> 6; sh[w] = mx; sh[4 + w] = mn; sh[8 + w] = sm; sh[12 + w] = n; }
  __syncthreads();
  mx = max(max(sh[0], sh[1]), max(sh[2], sh[3])); mn = min(min(sh[4], sh[5]), min(sh[6], sh[7]));
  sm = sh[8] + sh[9] + sh[10] + sh[11]; n = sh[12] + sh[13] + sh[14] + sh[15];
  return kind == 0 ? mx : kind == 1 ? mn : sm / n;
}
__global__ void gqa_stat_kernel(unsigned char* ws) {
  const unsigned long long* sp = (const unsigned long long*)(ws + STAMP_OFF);
  const unsigned ref = (unsigned)sp[0] - 100000u;
  __shared__ unsigned sh[16];
  const unsigned a = stat_one(sp, ref, K_STAT < 0 ? 0 : K_STAT, sh), b = stat_one(sp, ref, K_REF, sh);
  unsigned dl = K_STAT < 0 ? ((a - b) >> 20) : K_SCALE * (a - b);
  if (dl > 100000) dl = 100000;
  const unsigned long long t0 = __builtin_amdgcn_s_memrealtime();
  while (__builtin_amdgcn_s_memrealtime() - t0 < dl) __builtin_amdgcn_s_sleep(2);
}
extern "C" void kernel_launch(void* const* d_in, const int* in_sizes, int n_in,
                              void* d_out, int out_size, void* d_ws, size_t ws_size,
                              hipStream_t stream) {
  (void)in_sizes; (void)n_in; (void)out_size; (void)ws_size;
  gqa_decode_kernel<<<NGRP * NSPLIT, NTHR, 0, stream>>>((const u32x4*)d_in[0], (const unsigned char*)d_in[1], (const unsigned char*)d_in[2],
                                                        (unsigned short*)d_out, (unsigned char*)d_ws);
  gqa_stat_kernel<<<1, 256, 0, stream>>>((unsigned char*)d_ws);
}
```

```cpp
#define K_JP 6
#define K_TT 0
#define K_DEL 50
#define K_BACK 0
#define K_LEAD 50
#include <hip/hip_runtime.h>
#include <hip/hip_bf16.h>
#include <stdint.h>
#include <type_traits>
#include <utility>

#define K_RATE 7600
#define K_NTL 2
#define K_OWN 1
#define K_PFB 1
#define K_PFPOL " sc0 sc1"
#define K_ALLST 0
#define K_HOFF 0

typedef __attribute__((ext_vector_type(4))) unsigned u32x4;
typedef __attribute__((ext_vector_type(4))) float f32x4;
typedef __attribute__((ext_vector_type(2))) float f32x2;
typedef __attribute__((ext_vector_type(8))) __bf16 bf16x8;
typedef __attribute__((ext_vector_type(2))) __bf16 bf16x2;

constexpr int NB = 4, HKV = 16, SK = 4096, D = 128;
constexpr int NGRP = NB * HKV;
constexpr int NSPLIT = 4, WAVES = 4, NTHR = WAVES * 64;
constexpr int KPS = SK / NSPLIT;
constexpr int CH = 16384;
constexpr int NCH = KPS * D * 2 / CH;
constexpr int NTL = K_NTL, C0 = NCH - NTL;
constexpr int NST = NCH + 3 * NTL;
constexpr float C2 = 0.08838834764831845f * 1.4426950408889634f;
constexpr unsigned TAU = K_RATE ? (unsigned)(NGRP * NSPLIT * 16384.0 / (K_RATE * 1e9) * 1e8 * 256) : 0;

constexpr size_t WS_EPOCH = 0;
constexpr size_t WS_PART = 64 * 1024;
constexpr int GREC = 8 * 4096, SREC = 4 * 4096;

#define STAMP(i)
#define STAMP_FLUSH()

__device__ __forceinline__ float dot2(unsigned a, unsigned b, float acc) {
  return __builtin_amdgcn_fdot2_f32_bf16(__builtin_bit_cast(bf16x2, a), __builtin_bit_cast(bf16x2, b), acc, false);
}
template <int CTRL> __device__ __forceinline__ float dpp(float x) {
  return __builtin_bit_cast(float, __builtin_amdgcn_mov_dpp(__builtin_bit_cast(int, x), CTRL, 0xf, 0xf, true));
}
constexpr int XOR1 = 0xB1, XOR2 = 0x4E, ROR4 = 0x124, ROR8 = 0x128, ROR12 = 0x12C, IDENT = 0xE4;
__device__ __forceinline__ float xrow_max(float x) {
  auto s = __builtin_amdgcn_permlane16_swap(__float_as_uint(x), __float_as_uint(x), false, false);
  x = fmaxf(__uint_as_float(s[0]), __uint_as_float(s[1]));
  auto t = __builtin_amdgcn_permlane32_swap(__float_as_uint(x), __float_as_uint(x), false, false);
  return fmaxf(__uint_as_float(t[0]), __uint_as_float(t[1]));
}
__device__ __forceinline__ float xrow_sum(float x) {
  auto s = __builtin_amdgcn_permlane16_swap(__float_as_uint(x), __float_as_uint(x), false, false);
  x = __uint_as_float(s[0]) + __uint_as_float(s[1]);
  auto t = __builtin_amdgcn_permlane32_swap(__float_as_uint(x), __float_as_uint(x), false, false);
  return __uint_as_float(t[0]) + __uint_as_float(t[1]);
}
typedef __attribute__((ext_vector_type(4))) int rsrc_t;
__device__ __forceinline__ rsrc_t mkrsrc(const void* p, int bytes) {
  const unsigned long long a = (unsigned long long)p;
  return rsrc_t{(int)(unsigned)a, (int)(unsigned)(a >> 32) & 0xffff, bytes, 0x00020000};
}
template <int U> __device__ __forceinline__ void ldstream(rsrc_t r, int voff, int soff);
template <int U, int N> struct Landed;
#define RING(U, A, B, C, D) \
  template <> __device__ __forceinline__ void ldstream<U>(rsrc_t r, int voff, int soff) { \
    asm volatile("buffer_load_dwordx4 v[" #A ":" #D "], %0, %1, %2 offen nt" :: "v"(voff), "s"(r), "s"(soff) : "memory", "v" #A, "v" #B, "v" #C, "v" #D); \
    __builtin_amdgcn_sched_barrier(0); } \
  template <int N> struct Landed<U, N> { static __device__ __forceinline__ u32x4 go() { \
    u32x4 v; \
    asm volatile("s_waitcnt vmcnt(%1)" : "={v[" #A ":" #D "]}"(v) : "n"(N) : "memory"); \
    __builtin_amdgcn_sched_barrier(0); \
    return v; } };
RING(0, 64, 65, 66, 67) RING(1, 68, 69, 70, 71) RING(2, 72, 73, 74, 75) RING(3, 76, 77, 78, 79)
RING(4, 80, 81, 82, 83) RING(5, 84, 85, 86, 87) RING(6, 88, 89, 90, 91) RING(7, 92, 93, 94, 95)
__device__ __forceinline__ void pace(unsigned& tgt, unsigned tau) {
  asm volatile("s_add_u32 %0, %0, %1\n\t"
               "s_branch 1f\n"
               "0:\n\t"
               "s_sleep 1\n"
               "1:\n\t"
               "s_memrealtime s[96:97]\n\t"
               "s_waitcnt lgkmcnt(0)\n\t"
               "s_lshl_b32 s96, s96, 8\n\t"
               "s_sub_u32 s96, s96, %0\n\t"
               "s_cmp_lt_i32 s96, 0\n\t"
               "s_cbranch_scc1 0b"
               : "+s"(tgt) : "s"(tau) : "s96", "s97", "scc", "memory");
  __builtin_amdgcn_sched_barrier(0);
}
template <int R, int B> __device__ __forceinline__ unsigned place(unsigned old, unsigned src) {
  constexpr int n = (R - B) & 3;
  constexpr int ctrl = n == 0 ? IDENT : n == 1 ? ROR4 : n == 2 ? ROR8 : ROR12;
  return (unsigned)__builtin_amdgcn_update_dpp((int)old, (int)src, ctrl, 0xf, 1 << R, false);
}
template <int B> __device__ __forceinline__ void place4(u32x4& a, unsigned src) {
  a[0] = place<0, B>(a[0], src); a[1] = place<1, B>(a[1], src); a[2] = place<2, B>(a[2], src); a[3] = place<3, B>(a[3], src);
}
__device__ __forceinline__ f32x4 mfma(u32x4 a, u32x4 b, f32x4 c) {
  c = __builtin_amdgcn_mfma_f32_16x16x32_bf16(__builtin_bit_cast(bf16x8, a), __builtin_bit_cast(bf16x8, b), c, 0, 0, 0);
  asm volatile("" : "+a"(c));
  __builtin_amdgcn_sched_barrier(0);
  return c;
}
__device__ __forceinline__ void pace_if(unsigned& tgt, unsigned tau) {
  asm volatile("s_cmp_eq_u32 %1, 0\n\t"
               "s_cbranch_scc1 2f\n\t"
               "s_add_u32 %0, %0, %1\n\t"
               "s_branch 1f\n"
               "0:\n\t"
               "s_sleep 1\n"
               "1:\n\t"
               "s_memrealtime s[96:97]\n\t"
               "s_waitcnt lgkmcnt(0)\n\t"
               "s_lshl_b32 s96, s96, 8\n\t"
               "s_sub_u32 s96, s96, %0\n\t"
               "s_cmp_lt_i32 s96, 0\n\t"
               "s_cbranch_scc1 0b\n"
               "2:"
               : "+s"(tgt) : "s"(tau) : "s96", "s97", "scc", "memory");
  __builtin_amdgcn_sched_barrier(0);
}
#define LOOK3(A, B, C, OFF) "buffer_load_dwordx4 " A ", %[vo], %[rp], %[s1] offen" OFF " sc1\n\t" \
                            "buffer_load_dwordx4 " B ", %[vo], %[rp], %[s2] offen" OFF " sc1\n\t" \
                            "buffer_load_dwordx4 " C ", %[vo], %[rp], %[s3] offen" OFF " sc1\n\t"
#define LOOK_A LOOK3("v[100:103]", "v[104:107]", "v[108:111]", "")
#define LOOK_B LOOK3("v[112:115]", "v[116:119]", "v[120:123]", " offset:512")
#define LOOK_IN(VO, RP) [vo] "v"(VO), [rp] "s"(RP), [s1] "s"(4096), [s2] "s"(2 * 4096), [s3] "s"(3 * 4096)
#define LOOK_REGS "v100", "v101", "v102", "v103", "v104", "v105", "v106", "v107", "v108", "v109", "v110", "v111", \
                  "v112", "v113", "v114", "v115", "v116", "v117", "v118", "v119", "v120", "v121", "v122", "v123"
#define DIFF(D, a, b) "v_bitop3_b32 " D ", " a ", " b ", %[tag] bitop3:0x7e\n\t"
#define DIFF_A DIFF("%0", "v100", "v102") DIFF("%1", "v104", "v106") DIFF("%2", "v108", "v110") "v_or3_b32 %0, %0, %1, %2\n\t"
#define DIFF_B DIFF("%1", "v112", "v114") DIFF("%2", "v116", "v118") "v_or3_b32 %0, %0, %1, %2\n\t" DIFF("%1", "v120", "v122") "v_or_b32 %0, %0, %1\n\t"
#define TAKE_A "v_mov_b32 %0, v101\n\tv_mov_b32 %1, v103\n\tv_mov_b32 %2, v105\n\tv_mov_b32 %3, v107\n\tv_mov_b32 %4, v109\n\tv_mov_b32 %5, v111\n\t"
#define TAKE_B "v_mov_b32 %6, v113\n\tv_mov_b32 %7, v115\n\tv_mov_b32 %8, v117\n\tv_mov_b32 %9, v119\n\tv_mov_b32 %10, v121\n\tv_mov_b32 %11, v123\n\t"

constexpr int LDS_T = 0;
constexpr int LDS_O = WAVES * NST * 64;
constexpr int LDS_M = LDS_O + WAVES * 256;
constexpr int LDS_L = LDS_M + 8;
constexpr int LDS_N = 21 * 1024;
template <int I> using IC = std::integral_constant<int, I>;

__global__ void __launch_bounds__(NTHR)
gqa_decode_kernel(const u32x4* __restrict__ q, const unsigned char* kc, const unsigned char* vc,
                  unsigned short* __restrict__ out, unsigned char* __restrict__ ws
                  ) {
#pragma clang fp contract(off)
  __shared__ __attribute__((aligned(16))) float lds[LDS_N];
  const unsigned tau = TAU, lead = K_LEAD, del = K_DEL << 8, taut = TAU * K_TT / 100, hoff = K_HOFF * 256; const int jp = K_JP;
  STAMP(0);
  unsigned tgt = ((unsigned)__builtin_amdgcn_s_memrealtime() + lead) << 8;
  const int tid = threadIdx.x, lane = tid & 63;
  const int wave = __builtin_amdgcn_readfirstlane(tid >> 6);
  const int lin = (blockIdx.x & 7) * (NGRP * NSPLIT / 8) + (blockIdx.x >> 3);
  const int grp = lin / NSPLIT, split = lin % NSPLIT;
  const int c = lane & 15, hl = (lane >> 1) & 1;

  const size_t base = (size_t)lin * (KPS * D * 2);
  const unsigned char* kp = kc + base;
  const unsigned char* vp = vc + base;
  const int voff = tid * 16;

  unsigned* ep = reinterpret_cast<unsigned*>(ws + WS_EPOCH + (size_t)grp * 128);
  const unsigned char* pp = ws + WS_PART + (size_t)grp * GREC;
  u32x4 qa, qb;
  {
    const rsrc_t r = mkrsrc(q + grp * 32, 512);
    const int o = hl * 256 + c * 16;
    asm volatile("buffer_load_dwordx4 %0, %2, %4, 0 offen nt\n\tbuffer_load_dwordx4 %1, %3, %4, 0 offen nt"
                 : "=&v"(qa), "=&v"(qb) : "v"(o), "v"(o ^ 256), "s"(r) : "memory");
  }
  auto each = [](auto n, auto f) { [&]<int... U>(std::integer_sequence<int, U...>) { (f(IC<U>()), ...); }(std::make_integer_sequence<int, decltype(n)::value>()); };
  {
    const rsrc_t r = mkrsrc(kp, 2 * CH);
    each(IC<8>(), [&](auto u) { ldstream<u>(r, voff, u * 4096); });
  }
  unsigned tag;
  asm volatile("s_load_dword %0, %3, 0\n\ts_waitcnt vmcnt(8) lgkmcnt(0)\n\ts_add_u32 %0, %0, 0x9e3779b9"
               : "=&s"(tag), "+v"(qa), "+v"(qb) : "s"(ep) : "memory", "scc");
  float* sT = lds + LDS_T + wave * NST * 64 + lane;
  float mx = -INFINITY;
  const bool b3 = lane & 8, b2 = lane & 4;
#pragma unroll 1
  for (int it = 0; it < NCH / 2; ++it) {
    const rsrc_t r = mkrsrc(it + 1 < NCH / 2 ? kp + (size_t)(it + 1) * 2 * CH : vp, 2 * CH);
    each(IC<2>(), [&](auto a) {
      float z[4];
      each(IC<4>(), [&](auto b) {
        constexpr int u = a * 4 + b;
        float da = 0.f, db = 0.f;
        const u32x4 kv = Landed<u, 7>::go();
#pragma unroll
        for (int k = 0; k < 4; ++k) { da = dot2(kv[k], qa[k], da); db = dot2(kv[k], qb[k], db); }
        asm volatile("" : "+v"(da), "+v"(db));
        __builtin_amdgcn_sched_barrier(0);
        if (b == 0) pace(tgt, tau);
        ldstream<u>(r, voff, u * 4096);
        z[b] = da + dpp<XOR2>(db);
      });
      const float u0 = z[0] + dpp<ROR8>(z[0]), u2 = z[2] + dpp<ROR8>(z[2]);
      const float u1 = z[1] + dpp<ROR8>(z[1]), u3 = z[3] + dpp<ROR8>(z[3]);
      const float w0 = b3 ? u2 : u0, w1 = b3 ? u3 : u1;
      const float x0 = w0 + dpp<0x104>(w0);
      const float x1 = w1 + dpp<0x114>(w1);
      float t = b2 ? x1 : x0;
      t += dpp<XOR1>(t);
      mx = fmaxf(mx, t);
      sT[(it * 2 + a) * 64] = t;
      __builtin_amdgcn_sched_barrier(0);
    });
  }
  STAMP(1);
  auto wavemax = [](float x) { x = fmaxf(x, dpp<ROR4>(x)); x = fmaxf(x, dpp<ROR8>(x)); return xrow_max(x); };
  float mc = wavemax(mx) * C2;
  const unsigned hm = (lane & 1) ? 0xffff0000u : 0x0000ffffu;
  float l = 0.f;
  f32x4 acc = {0.f, 0.f, 0.f, 0.f};
  u32x4 A0 = {0u, 0u, 0u, 0u}, A1 = {0u, 0u, 0u, 0u};
  const rsrc_t rp = mkrsrc(pp, SREC), rs = mkrsrc(pp + SREC, SREC);
  const int soff = wave * 1024 + (lane >> 1) * 16;
  {
    const int o = (lane & 1) * 2;
    const float s0 = sT[(C0 + (o < NTL ? o : 0)) * 64], s1 = sT[(C0 + (o + 1 < NTL ? o + 1 : 0)) * 64];
    u32x4 rec = {tag, __float_as_uint(s0), tag, __float_as_uint(s1)};
    asm volatile("buffer_store_dwordx4 %0, %1, %2, %3 offen sc1\n\ts_nop 1"
                 :: "v"(rec), "v"(soff + (lane & 1) * (NTL > 2 ? 512 : SREC)), "s"(rs), "s"(split || K_ALLST ? split * 4096 : SREC) : "memory");
  }
  auto weight = [&](float t) {
    const float p = __builtin_amdgcn_exp2f(fmaf(t, C2, -mc));
    l += p;
    bf16x2 b; b[0] = (__bf16)p; b[1] = b[0];
    return __builtin_bit_cast(unsigned, b) & hm;
  };
  auto chunk = [&](auto h, auto w, auto next, unsigned P, rsrc_t r, int so, unsigned dt) {
    each(IC<4>(), [&](auto b) {
      constexpr int u = h * 4 + b;
      u32x4& A = (b & 1) ? A1 : A0;
      place4<b>(A, P);
      asm volatile("" : "+v"(A));
      acc = mfma(A, Landed<u, next ? w : w - b>::go(), acc);
      if (next == 1 && b == 0) pace(tgt, dt);
      if (next == 2 && b == 0) pace_if(tgt, dt);
      if (next) ldstream<u>(r, voff, so + b * 4096);
    });
  };
  auto pair = [&](int g, auto w) {
    const rsrc_t r = mkrsrc(vp + (size_t)(g + 2) * CH, 2 * CH);
    const unsigned P0 = weight(sT[g * 64]), P1 = weight(sT[(g + 1) * 64]);
    chunk(IC<0>(), w, IC<1>(), P0, r, 0, tau);
    chunk(IC<1>(), w, IC<1>(), P1, r, CH, tau);
  };
  auto stats = [&] {
    l += dpp<ROR4>(l); l += dpp<ROR8>(l); l = xrow_sum(l);
    if ((lane & 61) == 0) { lds[LDS_M + hl * 4 + wave] = mc; lds[LDS_L + hl * 4 + wave] = l; }
    __builtin_amdgcn_sched_barrier(0);
  };
  int so = (LDS_O + wave * 256 + c * 8 + (lane >> 4) * 2) * 4;
  asm volatile("" : "+v"(so));
  __builtin_assume((so & 7) == 0);
  auto spill = [&](auto mid) {
    *reinterpret_cast<f32x2*>((char*)lds + so) = f32x2{acc[0], acc[1]};
    *reinterpret_cast<f32x2*>((char*)lds + so + 512) = f32x2{acc[2], acc[3]};
    __builtin_amdgcn_sched_barrier(0);
    STAMP(2);
    mid(IC<0>());
    asm volatile("s_waitcnt lgkmcnt(0)\n\ts_barrier" ::: "memory");
    mid(IC<1>());
  };
  f32x4 fw;
  float lse;
  auto factors = [&] {
    const f32x4 tl = *reinterpret_cast<const f32x4*>(lds + LDS_L + (tid >> 7) * 4);
    const f32x4 tm = *reinterpret_cast<const f32x4*>(lds + LDS_M + (tid >> 7) * 4);
    const float M = fmaxf(fmaxf(tm[0], tm[1]), fmaxf(tm[2], tm[3]));
    float L = 0.f;
#pragma unroll
    for (int w = 0; w < WAVES; ++w) { fw[w] = __builtin_amdgcn_exp2f(tm[w] - M); L = fmaf(fw[w], tl[w], L); }
    fw *= __builtin_amdgcn_rcpf(L);
    lse = M + __builtin_amdgcn_logf(L);
  };
  constexpr int E0 = (C0 & ~1) - 2;
  pair(0, IC<8>());
#pragma unroll 1
  for (int g = 2; g < E0 - 2; g += 2) pair(g, IC<7>());
  const rsrc_t rg = mkrsrc(vc + (size_t)grp * (SK * D * 2), SK * D * 2);
  constexpr int H = C0 & 1;

  if (split) {
    pair(E0 - 2, IC<7>());
    STAMP(7);
    const int gl = ((split - 1) * 4 + wave) * 11 + lane - 32;
    const int po = lane < 32 ? split * (KPS * D * 2) + wave * 4096 + lane * 128 : lane < 43 && gl < 128 ? gl * 128 : 1 << 21;
    auto pull = [&](auto i) {
      asm volatile("buffer_load_dword v100, %0, %1, %2 offen" K_PFPOL
                   :: "v"(K_OWN && i == 0 && lane >= 32 ? 1 << 21 : po), "s"(rg), "s"((C0 + i) * CH) : "memory", "v100");
      __builtin_amdgcn_sched_barrier(0);
    };
    if (H) chunk(IC<0>(), IC<7>(), IC<1>(), weight(sT[E0 * 64]), mkrsrc(vp + (E0 + 2) * CH, CH), 0, tau);
    const unsigned P0 = weight(sT[(C0 - 2) * 64]), P1 = weight(sT[(C0 - 1) * 64]);
    stats();
    pace(tgt, tau + hoff);
    pull(IC<0>());
    chunk(IC<H>(), IC<8>(), IC<0>(), P0, rg, 0, 0);
    chunk(IC<1 - H>(), IC<4>(), IC<0>(), P1, rg, 0, 0);
    spill([&](auto m) { if (m == K_PFB) pull(IC<1>()); });
    factors();
    float on = 0.f;
#pragma unroll
    for (int w = 0; w < WAVES; ++w) on = fmaf(fw[w], lds[LDS_O + w * 256 + tid], on);
    u32x4 rec = {tag, __float_as_uint(on), tag, __float_as_uint(lse)};
    asm volatile("buffer_store_dwordx4 %0, %1, %2, %3 offen sc1\n\ts_nop 1" :: "v"(rec), "v"(voff), "s"(rp), "s"(split * 4096) : "memory");
    STAMP(3);
    if (K_PFB == 2) { pace(tgt, tau); pull(IC<1>()); } else tgt += tau;
    each(IC<NTL - 2>(), [&](auto i) { pace(tgt, tau); pull(IC<i + 2>()); });
    asm volatile("s_waitcnt vmcnt(0)" ::: "memory");
    STAMP(5);
    STAMP_FLUSH();
    return;
  }

  constexpr int NS = NTL > 2 ? 6 : 3;
  asm volatile(LOOK_A :: LOOK_IN(soff, rs) : "memory", LOOK_REGS);
  if (NTL > 2) asm volatile(LOOK_B :: LOOK_IN(soff, rs) : "memory", LOOK_REGS);
  pair(E0 - 2, IC<7 + NS>());
  {
    float s[12];
    unsigned spin = 1u << 18;
    asm volatile("s_waitcnt vmcnt(8)\n"
                 "0:\n\t"
                 DIFF_A
                 "v_cmp_ne_u32 vcc, 0, %0\n\t"
                 TAKE_A
                 "s_cmp_eq_u64 vcc, 0\n\t"
                 "s_cbranch_scc1 1f\n\t"
                 LOOK_A
                 "s_waitcnt vmcnt(0)\n\t"
                 "s_sub_u32 %[n], %[n], 1\n\t"
                 "s_cbranch_scc0 0b\n"
                 "1:"
                 : "=&v"(s[0]), "=&v"(s[1]), "=&v"(s[2]), "=&v"(s[3]), "=&v"(s[4]), "=&v"(s[5]),
                   "=&v"(s[6]), "=&v"(s[7]), "=&v"(s[8]), "=&v"(s[9]), "=&v"(s[10]), "=&v"(s[11]), [n] "+s"(spin)
                 : [tag] "s"(tag), LOOK_IN(soff, rs) : "memory", "vcc", "scc", LOOK_REGS);
    float m2 = -INFINITY;
#pragma unroll
    for (int i = 0; i < 3 * NTL; ++i) m2 = fmaxf(m2, s[i / 3 / 2 * 6 + i % 3 * 2 + (i / 3 & 1)]);
    m2 = fmaxf(mc, wavemax(m2) * C2);
    const float f = __builtin_amdgcn_exp2f(mc - m2), fo = dpp<XOR2>(f);
    const float f0 = hl ? fo : f, f1 = hl ? f : fo;
    mc = m2; l *= f;
    acc[0] *= f0; acc[1] *= f0; acc[2] *= f1; acc[3] *= f1;
    asm volatile("" : "+a"(acc));
    unsigned own[NCH - E0];
#pragma unroll
    for (int i = E0; i < NCH; ++i) own[i - E0] = weight(sT[i * 64]);
#pragma unroll
    for (int i = E0; i < NCH; ++i) sT[(i < C0 ? i : C0 + 4 * (i - C0)) * 64] = __uint_as_float(own[i - E0]);
#pragma unroll
    for (int i = 0; i < 3 * NTL; ++i) sT[(C0 + i / 3 * 4 + 1 + i % 3) * 64] = __uint_as_float(weight(s[i / 3 / 2 * 6 + i % 3 * 2 + (i / 3 & 1)]));
    stats();
  }
  asm volatile("s_waitcnt lgkmcnt(0)\n\ts_barrier" ::: "memory");
  factors();
  unsigned P0 = __float_as_uint(sT[(C0 - 2) * 64]), P1 = __float_as_uint(sT[(C0 - 1) * 64]);
  asm volatile("" : "+v"(fw), "+v"(lse), "+v"(P0), "+v"(P1));
  if (H) chunk(IC<0>(), IC<7>(), IC<1>(), __float_as_uint(sT[E0 * 64]), rg, (E0 + 2) * CH, tau);
  auto look = [&](int j) {
    asm volatile("s_cmp_lg_u32 %[j], %[jp]\n\ts_cbranch_scc1 1f\n\t" LOOK_A "1:"
                 :: [j] "s"(j), [jp] "s"(jp), LOOK_IN(voff, rp) : "memory", "scc", LOOK_REGS);
    __builtin_amdgcn_sched_barrier(0);
  };
  unsigned d0 = K_OWN ? tau : tau + del + taut, d1 = K_OWN ? del + 2 * taut : taut;
#pragma unroll 1
  for (int j = 0; j < 4 * NTL; j += 2) {
    const unsigned N0 = __float_as_uint(sT[(C0 + j) * 64]), N1 = __float_as_uint(sT[(C0 + 1 + j) * 64]);
    __builtin_amdgcn_sched_barrier(0);
    const int o = (j & 2) * (KPS * D * 2) + (C0 + (j >> 2)) * CH;
    look(j);
    chunk(IC<H>(), IC<7>(), IC<2>(), P0, rg, o, d0);
    look(j + 1);
    chunk(IC<1 - H>(), IC<7>(), IC<2>(), P1, rg, o + KPS * D * 2, d1);
    d0 = d1 = taut;
    P0 = N0; P1 = N1;
  }
  STAMP(7);
  static_assert(K_JP >= 0 && K_JP < 4 * NTL, "one look inside the loop");
  float rest;
  f32x4 g;
  unsigned long long bad;
  auto blend = [&](auto again) {
    float o1, l1, o2, l2, o3, l3;
    unsigned spin = again ? 1u << 18 : 0;
    asm volatile("0:\n\t"
                 "s_waitcnt vmcnt(%[w])\n\t"
                 DIFF_A
                 "v_cmp_ne_u32 %[bad], 0, %0\n\t"
                 TAKE_A
                 "s_cmp_eq_u64 %[bad], 0\n\t"
                 "s_cbranch_scc1 1f\n\t"
                 LOOK_A
                 "s_sub_u32 %[n], %[n], 1\n\t"
                 "s_cbranch_scc0 0b\n"
                 "1:"
                 : "=&v"(o1), "=&v"(l1), "=&v"(o2), "=&v"(l2), "=&v"(o3), "=&v"(l3), [n] "+s"(spin), [bad] "=&s"(bad)
                 : [tag] "s"(tag), [w] "n"(again ? 0 : 4), LOOK_IN(voff, rp) : "memory", "scc", LOOK_REGS);
    const float Mx = fmaxf(fmaxf(lse, l2), fmaxf(l1, l3));
    const float e0 = __builtin_amdgcn_exp2f(lse - Mx), e1 = __builtin_amdgcn_exp2f(l1 - Mx);
    const float e2 = __builtin_amdgcn_exp2f(l2 - Mx), e3 = __builtin_amdgcn_exp2f(l3 - Mx);
    const float r = __builtin_amdgcn_rcpf((e0 + e2) + (e1 + e3));
    rest = fmaf(e1, o1, fmaf(e2, o2, e3 * o3)) * r;
    g = fw * (e0 * r);
    asm volatile("" : "+v"(rest), "+v"(g));
    __builtin_amdgcn_sched_barrier(0);
  };
  chunk(IC<H>(), IC<7>(), IC<0>(), P0, rg, 0, 0);
  blend(IC<0>());
  chunk(IC<1 - H>(), IC<3>(), IC<0>(), P1, rg, 0, 0);
  spill([](auto) {});
  float x[WAVES];
#pragma unroll
  for (int w = 0; w < WAVES; ++w) x[w] = lds[LDS_O + w * 256 + tid];
  STAMP(3);
  if (bad) blend(IC<1>());
#pragma unroll
  for (int w = 0; w < WAVES; ++w) rest = fmaf(g[w], x[w], rest);
  out[grp * 256 + tid] = __builtin_bit_cast(unsigned short, (__bf16)rest);
  if (tid == 0) __hip_atomic_store(ep, tag, __ATOMIC_RELAXED, __HIP_MEMORY_SCOPE_AGENT);
  STAMP(4); STAMP_FLUSH();
}

extern "C" void kernel_launch(void* const* d_in, const int* in_sizes, int n_in,
                              void* d_out, int out_size, void* d_ws, size_t ws_size,
                              hipStream_t stream) {
  (void)in_sizes; (void)n_in; (void)out_size; (void)ws_size;
  gqa_decode_kernel<<<NGRP * NSPLIT, NTHR, 0, stream>>>((const u32x4*)d_in[0], (const unsigned char*)d_in[1], (const unsigned char*)d_in[2],
                                                        (unsigned short*)d_out, (unsigned char*)d_ws
                                                        );
}
```

```cpp
#define K_PROBE 0
#define K_JP 6
#define K_TT 0
#define K_DEL 50
#define K_BACK 0
#define K_LEAD 50
#include <hip/hip_runtime.h>
#include <hip/hip_bf16.h>
#include <stdint.h>
#include <type_traits>
#include <utility>

#define K_RATE 7600
#define K_NTL 2
#define K_OWN 1
#define K_PFB 1
#define K_PFPOL " sc0 sc1"
#define K_ALLST 0
#define K_HOFF 0

typedef __attribute__((ext_vector_type(4))) unsigned u32x4;
typedef __attribute__((ext_vector_type(4))) float f32x4;
typedef __attribute__((ext_vector_type(2))) float f32x2;
typedef __attribute__((ext_vector_type(8))) __bf16 bf16x8;
typedef __attribute__((ext_vector_type(2))) __bf16 bf16x2;

constexpr int NB = 4, HKV = 16, SK = 4096, D = 128;
constexpr int NGRP = NB * HKV;
constexpr int NSPLIT = 4, WAVES = 4, NTHR = WAVES * 64;
constexpr int KPS = SK / NSPLIT;
constexpr int CH = 16384;
constexpr int NCH = KPS * D * 2 / CH;
constexpr int NTL = K_NTL, C0 = NCH - NTL;
constexpr int NST = NCH + 3 * NTL;
constexpr float C2 = 0.08838834764831845f * 1.4426950408889634f;
constexpr unsigned TAU = K_RATE ? (unsigned)(NGRP * NSPLIT * 16384.0 / (K_RATE * 1e9) * 1e8 * 256) : 0;

constexpr size_t WS_EPOCH = 0;
constexpr size_t WS_PART = 64 * 1024;
constexpr int GREC = 8 * 4096, SREC = 4 * 4096;

#define STAMP(i)
#define STAMP_FLUSH()

__device__ __forceinline__ float dot2(unsigned a, unsigned b, float acc) {
  return __builtin_amdgcn_fdot2_f32_bf16(__builtin_bit_cast(bf16x2, a), __builtin_bit_cast(bf16x2, b), acc, false);
}
template <int CTRL> __device__ __forceinline__ float dpp(float x) {
  return __builtin_bit_cast(float, __builtin_amdgcn_mov_dpp(__builtin_bit_cast(int, x), CTRL, 0xf, 0xf, true));
}
constexpr int XOR1 = 0xB1, XOR2 = 0x4E, ROR4 = 0x124, ROR8 = 0x128, ROR12 = 0x12C, IDENT = 0xE4;
__device__ __forceinline__ float xrow_max(float x) {
  auto s = __builtin_amdgcn_permlane16_swap(__float_as_uint(x), __float_as_uint(x), false, false);
  x = fmaxf(__uint_as_float(s[0]), __uint_as_float(s[1]));
  auto t = __builtin_amdgcn_permlane32_swap(__float_as_uint(x), __float_as_uint(x), false, false);
  return fmaxf(__uint_as_float(t[0]), __uint_as_float(t[1]));
}
__device__ __forceinline__ float xrow_sum(float x) {
  auto s = __builtin_amdgcn_permlane16_swap(__float_as_uint(x), __float_as_uint(x), false, false);
  x = __uint_as_float(s[0]) + __uint_as_float(s[1]);
  auto t = __builtin_amdgcn_permlane32_swap(__float_as_uint(x), __float_as_uint(x), false, false);
  return __uint_as_float(t[0]) + __uint_as_float(t[1]);
}
typedef __attribute__((ext_vector_type(4))) int rsrc_t;
__device__ __forceinline__ rsrc_t mkrsrc(const void* p, int bytes) {
  const unsigned long long a = (unsigned long long)p;
  return rsrc_t{(int)(unsigned)a, (int)(unsigned)(a >> 32) & 0xffff, bytes, 0x00020000};
}
template <int U> __device__ __forceinline__ void ldstream(rsrc_t r, int voff, int soff);
template <int U, int N> struct Landed;
#define RING(U, A, B, C, D) \
  template <> __device__ __forceinline__ void ldstream<U>(rsrc_t r, int voff, int soff) { \
    asm volatile("buffer_load_dwordx4 v[" #A ":" #D "], %0, %1, %2 offen nt" :: "v"(voff), "s"(r), "s"(soff) : "memory", "v" #A, "v" #B, "v" #C, "v" #D); \
    __builtin_amdgcn_sched_barrier(0); } \
  template <int N> struct Landed<U, N> { static __device__ __forceinline__ u32x4 go() { \
    u32x4 v; \
    asm volatile("s_waitcnt vmcnt(%1)" : "={v[" #A ":" #D "]}"(v) : "n"(N) : "memory"); \
    __builtin_amdgcn_sched_barrier(0); \
    return v; } };
RING(0, 64, 65, 66, 67) RING(1, 68, 69, 70, 71) RING(2, 72, 73, 74, 75) RING(3, 76, 77, 78, 79)
RING(4, 80, 81, 82, 83) RING(5, 84, 85, 86, 87) RING(6, 88, 89, 90, 91) RING(7, 92, 93, 94, 95)
__device__ __forceinline__ void pace(unsigned& tgt, unsigned tau) {
  asm volatile("s_add_u32 %0, %0, %1\n\t"
               "s_branch 1f\n"
               "0:\n\t"
               "s_sleep 1\n"
               "1:\n\t"
               "s_memrealtime s[96:97]\n\t"
               "s_waitcnt lgkmcnt(0)\n\t"
               "s_lshl_b32 s96, s96, 8\n\t"
               "s_sub_u32 s96, s96, %0\n\t"
               "s_cmp_lt_i32 s96, 0\n\t"
               "s_cbranch_scc1 0b"
               : "+s"(tgt) : "s"(tau) : "s96", "s97", "scc", "memory");
  __builtin_amdgcn_sched_barrier(0);
}
template <int R, int B> __device__ __forceinline__ unsigned place(unsigned old, unsigned src) {
  constexpr int n = (R - B) & 3;
  constexpr int ctrl = n == 0 ? IDENT : n == 1 ? ROR4 : n == 2 ? ROR8 : ROR12;
  return (unsigned)__builtin_amdgcn_update_dpp((int)old, (int)src, ctrl, 0xf, 1 << R, false);
}
template <int B> __device__ __forceinline__ void place4(u32x4& a, unsigned src) {
  a[0] = place<0, B>(a[0], src); a[1] = place<1, B>(a[1], src); a[2] = place<2, B>(a[2], src); a[3] = place<3, B>(a[3], src);
}
__device__ __forceinline__ f32x4 mfma(u32x4 a, u32x4 b, f32x4 c) {
  c = __builtin_amdgcn_mfma_f32_16x16x32_bf16(__builtin_bit_cast(bf16x8, a), __builtin_bit_cast(bf16x8, b), c, 0, 0, 0);
  asm volatile("" : "+a"(c));
  __builtin_amdgcn_sched_barrier(0);
  return c;
}
__device__ __forceinline__ void pace_if(unsigned& tgt, unsigned tau) {
  asm volatile("s_cmp_eq_u32 %1, 0\n\t"
               "s_cbranch_scc1 2f\n\t"
               "s_add_u32 %0, %0, %1\n\t"
               "s_branch 1f\n"
               "0:\n\t"
               "s_sleep 1\n"
               "1:\n\t"
               "s_memrealtime s[96:97]\n\t"
               "s_waitcnt lgkmcnt(0)\n\t"
               "s_lshl_b32 s96, s96, 8\n\t"
               "s_sub_u32 s96, s96, %0\n\t"
               "s_cmp_lt_i32 s96, 0\n\t"
               "s_cbranch_scc1 0b\n"
               "2:"
               : "+s"(tgt) : "s"(tau) : "s96", "s97", "scc", "memory");
  __builtin_amdgcn_sched_barrier(0);
}
#define LOOK3(A, B, C, OFF) "buffer_load_dwordx4 " A ", %[vo], %[rp], %[s1] offen" OFF " sc1\n\t" \
                            "buffer_load_dwordx4 " B ", %[vo], %[rp], %[s2] offen" OFF " sc1\n\t" \
                            "buffer_load_dwordx4 " C ", %[vo], %[rp], %[s3] offen" OFF " sc1\n\t"
#define LOOK_A LOOK3("v[100:103]", "v[104:107]", "v[108:111]", "")
#define LOOK_B LOOK3("v[112:115]", "v[116:119]", "v[120:123]", " offset:512")
#define LOOK_IN(VO, RP) [vo] "v"(VO), [rp] "s"(RP), [s1] "s"(4096), [s2] "s"(2 * 4096), [s3] "s"(3 * 4096)
#define LOOK_REGS "v100", "v101", "v102", "v103", "v104", "v105", "v106", "v107", "v108", "v109", "v110", "v111", \
                  "v112", "v113", "v114", "v115", "v116", "v117", "v118", "v119", "v120", "v121", "v122", "v123"
#define DIFF(D, a, b) "v_bitop3_b32 " D ", " a ", " b ", %[tag] bitop3:0x7e\n\t"
#define DIFF_A DIFF("%0", "v100", "v102") DIFF("%1", "v104", "v106") DIFF("%2", "v108", "v110") "v_or3_b32 %0, %0, %1, %2\n\t"
#define DIFF_B DIFF("%1", "v112", "v114") DIFF("%2", "v116", "v118") "v_or3_b32 %0, %0, %1, %2\n\t" DIFF("%1", "v120", "v122") "v_or_b32 %0, %0, %1\n\t"
#define TAKE_A "v_mov_b32 %0, v101\n\tv_mov_b32 %1, v103\n\tv_mov_b32 %2, v105\n\tv_mov_b32 %3, v107\n\tv_mov_b32 %4, v109\n\tv_mov_b32 %5, v111\n\t"
#define TAKE_B "v_mov_b32 %6, v113\n\tv_mov_b32 %7, v115\n\tv_mov_b32 %8, v117\n\tv_mov_b32 %9, v119\n\tv_mov_b32 %10, v121\n\tv_mov_b32 %11, v123\n\t"

constexpr int LDS_T = 0;
constexpr int LDS_O = WAVES * NST * 64;
constexpr int LDS_M = LDS_O + WAVES * 256;
constexpr int LDS_L = LDS_M + 8;
constexpr int LDS_N = 21 * 1024;
template <int I> using IC = std::integral_constant<int, I>;

__global__ void __launch_bounds__(NTHR)
gqa_decode_kernel(const u32x4* __restrict__ q, const unsigned char* kc, const unsigned char* vc,
                  unsigned short* __restrict__ out, unsigned char* __restrict__ ws
                  ) {
#pragma clang fp contract(off)
  __shared__ __attribute__((aligned(16))) float lds[LDS_N];
  const unsigned tau = TAU, lead = K_LEAD, del = K_DEL << 8, taut = TAU * K_TT / 100, hoff = K_HOFF * 256; const int jp = K_JP;
  STAMP(0);
  const unsigned pt0 = (unsigned)__builtin_amdgcn_s_memrealtime();
  unsigned tgt = (pt0 + lead) << 8;
  asm volatile("" :: "s"(q), "s"(kc), "s"(vc), "s"(out), "s"(ws));
  __builtin_amdgcn_sched_barrier(0);
  const unsigned pt1 = (unsigned)__builtin_amdgcn_s_memrealtime();
  __builtin_amdgcn_sched_barrier(0);
  const int tid = threadIdx.x, lane = tid & 63;
  const int wave = __builtin_amdgcn_readfirstlane(tid >> 6);
  const int lin = (blockIdx.x & 7) * (NGRP * NSPLIT / 8) + (blockIdx.x >> 3);
  const int grp = lin / NSPLIT, split = lin % NSPLIT;
  const int c = lane & 15, hl = (lane >> 1) & 1;

  const size_t base = (size_t)lin * (KPS * D * 2);
  const unsigned char* kp = kc + base;
  const unsigned char* vp = vc + base;
  const int voff = tid * 16;

  unsigned* ep = reinterpret_cast<unsigned*>(ws + WS_EPOCH + (size_t)grp * 128);
  const unsigned char* pp = ws + WS_PART + (size_t)grp * GREC;
  u32x4 qa, qb;
  {
    const rsrc_t r = mkrsrc(q + grp * 32, 512);
    const int o = hl * 256 + c * 16;
    asm volatile("buffer_load_dwordx4 %0, %2, %4, 0 offen nt\n\tbuffer_load_dwordx4 %1, %3, %4, 0 offen nt"
                 : "=&v"(qa), "=&v"(qb) : "v"(o), "v"(o ^ 256), "s"(r) : "memory");
  }
  auto each = [](auto n, auto f) { [&]<int... U>(std::integer_sequence<int, U...>) { (f(IC<U>()), ...); }(std::make_integer_sequence<int, decltype(n)::value>()); };
  {
    const rsrc_t r = mkrsrc(kp, 2 * CH);
    each(IC<8>(), [&](auto u) { ldstream<u>(r, voff, u * 4096); });
  }
  unsigned tag;
  asm volatile("s_load_dword %0, %3, 0\n\ts_waitcnt vmcnt(8) lgkmcnt(0)\n\ts_add_u32 %0, %0, 0x9e3779b9"
               : "=&s"(tag), "+v"(qa), "+v"(qb) : "s"(ep) : "memory", "scc");
  __builtin_amdgcn_sched_barrier(0);
  const unsigned pt2 = (unsigned)__builtin_amdgcn_s_memrealtime();
  asm volatile("s_waitcnt vmcnt(7)" ::: "memory");
  const unsigned pt3 = (unsigned)__builtin_amdgcn_s_memrealtime();
  __builtin_amdgcn_sched_barrier(0);
  float* sT = lds + LDS_T + wave * NST * 64 + lane;
  float mx = -INFINITY;
  const bool b3 = lane & 8, b2 = lane & 4;
#pragma unroll 1
  for (int it = 0; it < NCH / 2; ++it) {
    const rsrc_t r = mkrsrc(it + 1 < NCH / 2 ? kp + (size_t)(it + 1) * 2 * CH : vp, 2 * CH);
    each(IC<2>(), [&](auto a) {
      float z[4];
      each(IC<4>(), [&](auto b) {
        constexpr int u = a * 4 + b;
        float da = 0.f, db = 0.f;
        const u32x4 kv = Landed<u, 7>::go();
#pragma unroll
        for (int k = 0; k < 4; ++k) { da = dot2(kv[k], qa[k], da); db = dot2(kv[k], qb[k], db); }
        asm volatile("" : "+v"(da), "+v"(db));
        __builtin_amdgcn_sched_barrier(0);
        if (b == 0) pace(tgt, tau);
        ldstream<u>(r, voff, u * 4096);
        z[b] = da + dpp<XOR2>(db);
      });
      const float u0 = z[0] + dpp<ROR8>(z[0]), u2 = z[2] + dpp<ROR8>(z[2]);
      const float u1 = z[1] + dpp<ROR8>(z[1]), u3 = z[3] + dpp<ROR8>(z[3]);
      const float w0 = b3 ? u2 : u0, w1 = b3 ? u3 : u1;
      const float x0 = w0 + dpp<0x104>(w0);
      const float x1 = w1 + dpp<0x114>(w1);
      float t = b2 ? x1 : x0;
      t += dpp<XOR1>(t);
      mx = fmaxf(mx, t);
      sT[(it * 2 + a) * 64] = t;
      __builtin_amdgcn_sched_barrier(0);
    });
  }
  __builtin_amdgcn_sched_barrier(0);
  const unsigned pt4 = (unsigned)__builtin_amdgcn_s_memrealtime();
  __builtin_amdgcn_sched_barrier(0);
  auto wavemax = [](float x) { x = fmaxf(x, dpp<ROR4>(x)); x = fmaxf(x, dpp<ROR8>(x)); return xrow_max(x); };
  float mc = wavemax(mx) * C2;
  const unsigned hm = (lane & 1) ? 0xffff0000u : 0x0000ffffu;
  float l = 0.f;
  f32x4 acc = {0.f, 0.f, 0.f, 0.f};
  u32x4 A0 = {0u, 0u, 0u, 0u}, A1 = {0u, 0u, 0u, 0u};
  const rsrc_t rp = mkrsrc(pp, SREC), rs = mkrsrc(pp + SREC, SREC);
  const int soff = wave * 1024 + (lane >> 1) * 16;
  {
    const int o = (lane & 1) * 2;
    const float s0 = sT[(C0 + (o < NTL ? o : 0)) * 64], s1 = sT[(C0 + (o + 1 < NTL ? o + 1 : 0)) * 64];
    u32x4 rec = {tag, __float_as_uint(s0), tag, __float_as_uint(s1)};
    asm volatile("buffer_store_dwordx4 %0, %1, %2, %3 offen sc1\n\ts_nop 1"
                 :: "v"(rec), "v"(soff + (lane & 1) * (NTL > 2 ? 512 : SREC)), "s"(rs), "s"(split || K_ALLST ? split * 4096 : SREC) : "memory");
  }
  auto weight = [&](float t) {
    const float p = __builtin_amdgcn_exp2f(fmaf(t, C2, -mc));
    l += p;
    bf16x2 b; b[0] = (__bf16)p; b[1] = b[0];
    return __builtin_bit_cast(unsigned, b) & hm;
  };
  auto chunk = [&](auto h, auto w, auto next, unsigned P, rsrc_t r, int so, unsigned dt) {
    each(IC<4>(), [&](auto b) {
      constexpr int u = h * 4 + b;
      u32x4& A = (b & 1) ? A1 : A0;
      place4<b>(A, P);
      asm volatile("" : "+v"(A));
      acc = mfma(A, Landed<u, next ? w : w - b>::go(), acc);
      if (next == 1 && b == 0) pace(tgt, dt);
      if (next == 2 && b == 0) pace_if(tgt, dt);
      if (next) ldstream<u>(r, voff, so + b * 4096);
    });
  };
  auto pair = [&](int g, auto w) {
    const rsrc_t r = mkrsrc(vp + (size_t)(g + 2) * CH, 2 * CH);
    const unsigned P0 = weight(sT[g * 64]), P1 = weight(sT[(g + 1) * 64]);
    chunk(IC<0>(), w, IC<1>(), P0, r, 0, tau);
    chunk(IC<1>(), w, IC<1>(), P1, r, CH, tau);
  };
  auto stats = [&] {
    l += dpp<ROR4>(l); l += dpp<ROR8>(l); l = xrow_sum(l);
    if ((lane & 61) == 0) { lds[LDS_M + hl * 4 + wave] = mc; lds[LDS_L + hl * 4 + wave] = l; }
    __builtin_amdgcn_sched_barrier(0);
  };
  int so = (LDS_O + wave * 256 + c * 8 + (lane >> 4) * 2) * 4;
  asm volatile("" : "+v"(so));
  __builtin_assume((so & 7) == 0);
  auto spill = [&](auto mid) {
    *reinterpret_cast<f32x2*>((char*)lds + so) = f32x2{acc[0], acc[1]};
    *reinterpret_cast<f32x2*>((char*)lds + so + 512) = f32x2{acc[2], acc[3]};
    __builtin_amdgcn_sched_barrier(0);
    STAMP(2);
    mid(IC<0>());
    asm volatile("s_waitcnt lgkmcnt(0)\n\ts_barrier" ::: "memory");
    mid(IC<1>());
  };
  f32x4 fw;
  float lse;
  auto factors = [&] {
    const f32x4 tl = *reinterpret_cast<const f32x4*>(lds + LDS_L + (tid >> 7) * 4);
    const f32x4 tm = *reinterpret_cast<const f32x4*>(lds + LDS_M + (tid >> 7) * 4);
    const float M = fmaxf(fmaxf(tm[0], tm[1]), fmaxf(tm[2], tm[3]));
    float L = 0.f;
#pragma unroll
    for (int w = 0; w < WAVES; ++w) { fw[w] = __builtin_amdgcn_exp2f(tm[w] - M); L = fmaf(fw[w], tl[w], L); }
    fw *= __builtin_amdgcn_rcpf(L);
    lse = M + __builtin_amdgcn_logf(L);
  };
  constexpr int E0 = (C0 & ~1) - 2;
  pair(0, IC<8>());
#pragma unroll 1
  for (int g = 2; g < E0 - 2; g += 2) pair(g, IC<7>());
  const rsrc_t rg = mkrsrc(vc + (size_t)grp * (SK * D * 2), SK * D * 2);
  constexpr int H = C0 & 1;

  if (split) {
    pair(E0 - 2, IC<7>());
    STAMP(7);
    const int gl = ((split - 1) * 4 + wave) * 11 + lane - 32;
    const int po = lane < 32 ? split * (KPS * D * 2) + wave * 4096 + lane * 128 : lane < 43 && gl < 128 ? gl * 128 : 1 << 21;
    auto pull = [&](auto i) {
      asm volatile("buffer_load_dword v100, %0, %1, %2 offen" K_PFPOL
                   :: "v"(K_OWN && i == 0 && lane >= 32 ? 1 << 21 : po), "s"(rg), "s"((C0 + i) * CH) : "memory", "v100");
      __builtin_amdgcn_sched_barrier(0);
    };
    if (H) chunk(IC<0>(), IC<7>(), IC<1>(), weight(sT[E0 * 64]), mkrsrc(vp + (E0 + 2) * CH, CH), 0, tau);
    const unsigned P0 = weight(sT[(C0 - 2) * 64]), P1 = weight(sT[(C0 - 1) * 64]);
    stats();
    pace(tgt, tau + hoff);
    pull(IC<0>());
    chunk(IC<H>(), IC<8>(), IC<0>(), P0, rg, 0, 0);
    chunk(IC<1 - H>(), IC<4>(), IC<0>(), P1, rg, 0, 0);
    spill([&](auto m) { if (m == K_PFB) pull(IC<1>()); });
    factors();
    float on = 0.f;
#pragma unroll
    for (int w = 0; w < WAVES; ++w) on = fmaf(fw[w], lds[LDS_O + w * 256 + tid], on);
    u32x4 rec = {tag, __float_as_uint(on), tag, __float_as_uint(lse)};
    asm volatile("buffer_store_dwordx4 %0, %1, %2, %3 offen sc1\n\ts_nop 1" :: "v"(rec), "v"(voff), "s"(rp), "s"(split * 4096) : "memory");
    STAMP(3);
    if (K_PFB == 2) { pace(tgt, tau); pull(IC<1>()); } else tgt += tau;
    each(IC<NTL - 2>(), [&](auto i) { pace(tgt, tau); pull(IC<i + 2>()); });
    asm volatile("s_waitcnt vmcnt(0)" ::: "memory");
    STAMP(5);
    STAMP_FLUSH();
    return;
  }

  constexpr int NS = NTL > 2 ? 6 : 3;
  asm volatile(LOOK_A :: LOOK_IN(soff, rs) : "memory", LOOK_REGS);
  if (NTL > 2) asm volatile(LOOK_B :: LOOK_IN(soff, rs) : "memory", LOOK_REGS);
  pair(E0 - 2, IC<7 + NS>());
  {
    float s[12];
    unsigned spin = 1u << 18;
    asm volatile("s_waitcnt vmcnt(8)\n"
                 "0:\n\t"
                 DIFF_A
                 "v_cmp_ne_u32 vcc, 0, %0\n\t"
                 TAKE_A
                 "s_cmp_eq_u64 vcc, 0\n\t"
                 "s_cbranch_scc1 1f\n\t"
                 LOOK_A
                 "s_waitcnt vmcnt(0)\n\t"
                 "s_sub_u32 %[n], %[n], 1\n\t"
                 "s_cbranch_scc0 0b\n"
                 "1:"
                 : "=&v"(s[0]), "=&v"(s[1]), "=&v"(s[2]), "=&v"(s[3]), "=&v"(s[4]), "=&v"(s[5]),
                   "=&v"(s[6]), "=&v"(s[7]), "=&v"(s[8]), "=&v"(s[9]), "=&v"(s[10]), "=&v"(s[11]), [n] "+s"(spin)
                 : [tag] "s"(tag), LOOK_IN(soff, rs) : "memory", "vcc", "scc", LOOK_REGS);
    float m2 = -INFINITY;
#pragma unroll
    for (int i = 0; i < 3 * NTL; ++i) m2 = fmaxf(m2, s[i / 3 / 2 * 6 + i % 3 * 2 + (i / 3 & 1)]);
    m2 = fmaxf(mc, wavemax(m2) * C2);
    const float f = __builtin_amdgcn_exp2f(mc - m2), fo = dpp<XOR2>(f);
    const float f0 = hl ? fo : f, f1 = hl ? f : fo;
    mc = m2; l *= f;
    acc[0] *= f0; acc[1] *= f0; acc[2] *= f1; acc[3] *= f1;
    asm volatile("" : "+a"(acc));
    unsigned own[NCH - E0];
#pragma unroll
    for (int i = E0; i < NCH; ++i) own[i - E0] = weight(sT[i * 64]);
#pragma unroll
    for (int i = E0; i < NCH; ++i) sT[(i < C0 ? i : C0 + 4 * (i - C0)) * 64] = __uint_as_float(own[i - E0]);
#pragma unroll
    for (int i = 0; i < 3 * NTL; ++i) sT[(C0 + i / 3 * 4 + 1 + i % 3) * 64] = __uint_as_float(weight(s[i / 3 / 2 * 6 + i % 3 * 2 + (i / 3 & 1)]));
    stats();
  }
  asm volatile("s_waitcnt lgkmcnt(0)\n\ts_barrier" ::: "memory");
  factors();
  unsigned P0 = __float_as_uint(sT[(C0 - 2) * 64]), P1 = __float_as_uint(sT[(C0 - 1) * 64]);
  asm volatile("" : "+v"(fw), "+v"(lse), "+v"(P0), "+v"(P1));
  if (H) chunk(IC<0>(), IC<7>(), IC<1>(), __float_as_uint(sT[E0 * 64]), rg, (E0 + 2) * CH, tau);
  auto look = [&](int j) {
    asm volatile("s_cmp_lg_u32 %[j], %[jp]\n\ts_cbranch_scc1 1f\n\t" LOOK_A "1:"
                 :: [j] "s"(j), [jp] "s"(jp), LOOK_IN(voff, rp) : "memory", "scc", LOOK_REGS);
    __builtin_amdgcn_sched_barrier(0);
  };
  unsigned d0 = K_OWN ? tau : tau + del + taut, d1 = K_OWN ? del + 2 * taut : taut;
#pragma unroll 1
  for (int j = 0; j < 4 * NTL; j += 2) {
    const unsigned N0 = __float_as_uint(sT[(C0 + j) * 64]), N1 = __float_as_uint(sT[(C0 + 1 + j) * 64]);
    __builtin_amdgcn_sched_barrier(0);
    const int o = (j & 2) * (KPS * D * 2) + (C0 + (j >> 2)) * CH;
    look(j);
    chunk(IC<H>(), IC<7>(), IC<2>(), P0, rg, o, d0);
    look(j + 1);
    chunk(IC<1 - H>(), IC<7>(), IC<2>(), P1, rg, o + KPS * D * 2, d1);
    d0 = d1 = taut;
    P0 = N0; P1 = N1;
  }
  STAMP(7);
  static_assert(K_JP >= 0 && K_JP < 4 * NTL, "one look inside the loop");
  float rest;
  f32x4 g;
  unsigned long long bad;
  auto blend = [&](auto again) {
    float o1, l1, o2, l2, o3, l3;
    unsigned spin = again ? 1u << 18 : 0;
    asm volatile("0:\n\t"
                 "s_waitcnt vmcnt(%[w])\n\t"
                 DIFF_A
                 "v_cmp_ne_u32 %[bad], 0, %0\n\t"
                 TAKE_A
                 "s_cmp_eq_u64 %[bad], 0\n\t"
                 "s_cbranch_scc1 1f\n\t"
                 LOOK_A
                 "s_sub_u32 %[n], %[n], 1\n\t"
                 "s_cbranch_scc0 0b\n"
                 "1:"
                 : "=&v"(o1), "=&v"(l1), "=&v"(o2), "=&v"(l2), "=&v"(o3), "=&v"(l3), [n] "+s"(spin), [bad] "=&s"(bad)
                 : [tag] "s"(tag), [w] "n"(again ? 0 : 4), LOOK_IN(voff, rp) : "memory", "scc", LOOK_REGS);
    const float Mx = fmaxf(fmaxf(lse, l2), fmaxf(l1, l3));
    const float e0 = __builtin_amdgcn_exp2f(lse - Mx), e1 = __builtin_amdgcn_exp2f(l1 - Mx);
    const float e2 = __builtin_amdgcn_exp2f(l2 - Mx), e3 = __builtin_amdgcn_exp2f(l3 - Mx);
    const float r = __builtin_amdgcn_rcpf((e0 + e2) + (e1 + e3));
    rest = fmaf(e1, o1, fmaf(e2, o2, e3 * o3)) * r;
    g = fw * (e0 * r);
    asm volatile("" : "+v"(rest), "+v"(g));
    __builtin_amdgcn_sched_barrier(0);
  };
  chunk(IC<H>(), IC<7>(), IC<0>(), P0, rg, 0, 0);
  blend(IC<0>());
  chunk(IC<1 - H>(), IC<3>(), IC<0>(), P1, rg, 0, 0);
  spill([](auto) {});
  float x[WAVES];
#pragma unroll
  for (int w = 0; w < WAVES; ++w) x[w] = lds[LDS_O + w * 256 + tid];
  STAMP(3);
  if (bad) blend(IC<1>());
#pragma unroll
  for (int w = 0; w < WAVES; ++w) rest = fmaf(g[w], x[w], rest);
  out[grp * 256 + tid] = __builtin_bit_cast(unsigned short, (__bf16)rest);
  if (tid == 0) __hip_atomic_store(ep, tag, __ATOMIC_RELAXED, __HIP_MEMORY_SCOPE_AGENT);
  {
    const unsigned pe = (unsigned)__builtin_amdgcn_s_memrealtime();
    unsigned ex = K_PROBE == 1 ? (pt1 - pt0) * 10 : K_PROBE == 2 ? (pt2 - pt1) * 10 : K_PROBE == 3 ? (pt3 - pt1) * 10 : K_PROBE == 4 ? (pt4 - pt0) : K_PROBE == 5 ? pe - pt0 : 0;
    if (ex > 20000) ex = 20000;
    unsigned t = pe << 8;
    pace(t, ex << 8);
  }
}

extern "C" void kernel_launch(void* const* d_in, const int* in_sizes, int n_in,
                              void* d_out, int out_size, void* d_ws, size_t ws_size,
                              hipStream_t stream) {
  (void)in_sizes; (void)n_in; (void)out_size; (void)ws_size;
  gqa_decode_kernel<<<NGRP * NSPLIT, NTHR, 0, stream>>>((const u32x4*)d_in[0], (const unsigned char*)d_in[1], (const unsigned char*)d_in[2],
                                                        (unsigned short*)d_out, (unsigned char*)d_ws
                                                        );
}
```

```cpp
#define K_STAT -1
#define K_SCALE 1
#include <hip/hip_runtime.h>
#include <hip/hip_bf16.h>
#include <stdint.h>
#include <type_traits>
#include <utility>

typedef __attribute__((ext_vector_type(4))) unsigned u32x4;
typedef __attribute__((ext_vector_type(2))) unsigned u32x2;
typedef __attribute__((ext_vector_type(4))) float f32x4;
typedef __attribute__((ext_vector_type(2))) float f32x2;
typedef __attribute__((ext_vector_type(8))) __bf16 bf16x8;
typedef __attribute__((ext_vector_type(2))) __bf16 bf16x2;
typedef __attribute__((ext_vector_type(4))) int rsrc_t;

constexpr int NB = 4, HKV = 16, SK = 4096, D = 128;
constexpr int NGRP = NB * HKV;
constexpr int NSPLIT = 4, WAVES = 4, NTHR = WAVES * 64;
constexpr int KPS = SK / NSPLIT;
constexpr int SPB = KPS * D * 2;
constexpr int CH = 16384;
constexpr int NCH = SPB / CH;
constexpr int CL = NCH - 1;
constexpr int NST = 24;
constexpr float C2 = 0.08838834764831845f * 1.4426950408889634f;

constexpr unsigned TAU = (unsigned)(NGRP * NSPLIT * 16384.0 / 7700e9 * 1e8 * 256);
constexpr unsigned LEAD = 25, FIRST_DATA = 5;
constexpr unsigned DEL = 30 << 8;
constexpr int CODE_LINES = 128;

constexpr size_t WS_S = 512u << 10, WS_O = 1u << 20;
constexpr int SW = 192, SS = WAVES * SW, SREC = 3 * SS, OS = 2048, OREC = 3 * OS;

#define STAMP_OFF (8u << 20)
#define STAMP_N 8
#define STAMP_NW 1024
#define STAMP(i) do { __builtin_amdgcn_sched_barrier(0); unsigned long long t_ = __builtin_amdgcn_s_memrealtime(); stampv[i] = t_; __builtin_amdgcn_sched_barrier(0); } while (0)
#define STAMP_FLUSH() do { if (lane == 0) { unsigned long long* sp_ = (unsigned long long*)(ws + STAMP_OFF) + ((size_t)blockIdx.x * WAVES + wave) * 8; for (int i_ = 0; i_ < STAMP_N; ++i_) sp_[i_] = stampv[i_]; } } while (0)
__device__ __forceinline__ float dot2(unsigned a, unsigned b, float acc) {
  return __builtin_amdgcn_fdot2_f32_bf16(__builtin_bit_cast(bf16x2, a), __builtin_bit_cast(bf16x2, b), acc, false);
}
template <int CTRL> __device__ __forceinline__ float dpp(float x) {
  return __builtin_bit_cast(float, __builtin_amdgcn_mov_dpp(__builtin_bit_cast(int, x), CTRL, 0xf, 0xf, true));
}
constexpr int XOR1 = 0xB1, XOR2 = 0x4E, ROR4 = 0x124, ROR8 = 0x128, ROR12 = 0x12C, IDENT = 0xE4;
#define STEP(OP, HOW) "s_nop 1\n\t" OP "_dpp %0, %0, %0 " HOW " row_mask:0xf bank_mask:0xf\n\t"
#define SWAP(OP, N) "v_mov_b32 %1, %0\n\ts_nop 1\n\tv_permlane" N "_swap_b32 %0, %1\n\t" OP " %0, %0, %1\n\t"
#define ACROSS(NAME, OP) \
  __device__ __forceinline__ float NAME(float x) { \
    float y; \
    asm(STEP(OP, "quad_perm:[1,0,3,2]") STEP(OP, "row_ror:4") STEP(OP, "row_ror:8") SWAP(OP, "16") SWAP(OP, "32") : "+v"(x), "=&v"(y)); \
    return x; }
ACROSS(head_max, "v_max_f32") ACROSS(head_sum, "v_add_f32")
__device__ __forceinline__ rsrc_t mkrsrc(const void* p, int bytes) {
  const unsigned long long a = (unsigned long long)p;
  return rsrc_t{(int)(unsigned)a, (int)(unsigned)(a >> 32) & 0xffff, bytes, 0x00020000};
}
template <int U> __device__ __forceinline__ void ldstream(rsrc_t r, int voff, int soff);
template <int U, int N> struct Landed;
#define RING(U, A, B, C, D) \
  template <> __device__ __forceinline__ void ldstream<U>(rsrc_t r, int voff, int soff) { \
    asm volatile("buffer_load_dwordx4 v[" #A ":" #D "], %0, %1, %2 offen nt" :: "v"(voff), "s"(r), "s"(soff) : "memory", "v" #A, "v" #B, "v" #C, "v" #D); \
    __builtin_amdgcn_sched_barrier(0); } \
  template <int N> struct Landed<U, N> { static __device__ __forceinline__ u32x4 go() { \
    u32x4 v; \
    asm volatile("s_waitcnt vmcnt(%1)" : "={v[" #A ":" #D "]}"(v) : "n"(N) : "memory"); \
    __builtin_amdgcn_sched_barrier(0); \
    return v; } };
RING(0, 64, 65, 66, 67) RING(1, 68, 69, 70, 71) RING(2, 72, 73, 74, 75) RING(3, 76, 77, 78, 79)
RING(4, 80, 81, 82, 83) RING(5, 84, 85, 86, 87) RING(6, 88, 89, 90, 91) RING(7, 92, 93, 94, 95)
__device__ __forceinline__ void pace(unsigned& tgt, unsigned dt) {
  asm volatile("s_add_u32 %0, %0, %1\n\t"
               "s_branch 1f\n"
               "0:\n\t"
               "s_sleep 1\n"
               "1:\n\t"
               "s_memrealtime s[96:97]\n\t"
               "s_waitcnt lgkmcnt(0)\n\t"
               "s_lshl_b32 s96, s96, 8\n\t"
               "s_sub_u32 s96, s96, %0\n\t"
               "s_cmp_lt_i32 s96, 0\n\t"
               "s_cbranch_scc1 0b"
               : "+s"(tgt) : "s"(dt) : "s96", "s97", "scc", "memory");
  __builtin_amdgcn_sched_barrier(0);
}
template <int R, int B> __device__ __forceinline__ unsigned place(unsigned old, unsigned src) {
  constexpr int n = (R - B) & 3;
  constexpr int ctrl = n == 0 ? IDENT : n == 1 ? ROR4 : n == 2 ? ROR8 : ROR12;
  return (unsigned)__builtin_amdgcn_update_dpp((int)old, (int)src, ctrl, 0xf, 1 << R, false);
}
template <int B> __device__ __forceinline__ void place4(u32x4& a, unsigned src) {
  a[0] = place<0, B>(a[0], src); a[1] = place<1, B>(a[1], src); a[2] = place<2, B>(a[2], src); a[3] = place<3, B>(a[3], src);
}
__device__ __forceinline__ f32x4 mfma(u32x4 a, u32x4 b, f32x4 c) {
  c = __builtin_amdgcn_mfma_f32_16x16x32_bf16(__builtin_bit_cast(bf16x8, a), __builtin_bit_cast(bf16x8, b), c, 0, 0, 0);
  asm volatile("" : "+a"(c));
  __builtin_amdgcn_sched_barrier(0);
  return c;
}
#define LOOK(N, A, B, C) "buffer_load_dwordx" #N " v[100:" #A "], %[vo], %[rp], 0 offen sc1\n\t" \
                         "buffer_load_dwordx" #N " v[104:" #B "], %[vo], %[rp], %[s2] offen sc1\n\t" \
                         "buffer_load_dwordx" #N " v[108:" #C "], %[vo], %[rp], %[s3] offen sc1\n\t"
#define LOOK4 LOOK(4, 103, 107, 111)
#define LOOK2 LOOK(2, 101, 105, 109)
#define LOOK_IN [vo] "v"(vo), [rp] "s"(r), [s2] "s"(step), [s3] "s"(2 * step)
#define LOOK_REGS "v100", "v101", "v102", "v103", "v104", "v105", "v106", "v107", "v108", "v109", "v110", "v111"
template <int N> __device__ __forceinline__ void look(rsrc_t r, int vo, int step) {
  if (N == 4) asm volatile(LOOK4 :: LOOK_IN : "memory", LOOK_REGS);
  else asm volatile(LOOK2 :: LOOK_IN : "memory", LOOK_REGS);
  __builtin_amdgcn_sched_barrier(0);
}
__device__ __forceinline__ void pull(rsrc_t r, int vo, int so) {
  asm volatile("buffer_load_dword v100, %0, %1, %2 offen sc0 sc1" :: "v"(vo), "s"(r), "s"(so) : "memory", "v100");
  __builtin_amdgcn_sched_barrier(0);
}
__device__ __forceinline__ void post(unsigned tag, float a, float b, rsrc_t r, int vo) {
  const u32x4 rec = {tag, __float_as_uint(a), tag, __float_as_uint(b)};
  asm volatile("buffer_store_dwordx4 %0, %1, %2, 0 offen sc1\n\ts_nop 1" :: "v"(rec), "v"(vo), "s"(r) : "memory");
}
#define FIRST(A, B) "v_bitop3_b32 %[d], v" #A ", v" #B ", %[tag] bitop3:0x7e\n\t"
#define ALSO(A) "v_bitop3_b32 %[d], %[d], v" #A ", %[tag] bitop3:0xf6\n\t"
#define TAKE(CHECK, AGAIN) \
  asm volatile("s_waitcnt vmcnt(%[w])\n" \
               "0:\n\t" CHECK \
               "v_cmp_ne_u32 vcc, 0, %[d]\n\t" \
               "s_cbranch_vccz 1f\n\t" \
               "s_sub_u32 %[n], %[n], 1\n\t" \
               "s_cbranch_scc1 1f\n\t" AGAIN \
               "s_waitcnt vmcnt(0)\n\t" \
               "s_branch 0b\n" \
               "1:" \
               : "=&{v[100:103]}"(a), "=&{v[104:107]}"(b), "=&{v[108:111]}"(c), [d] "=&v"(d), [n] "+s"(spin) \
               : [tag] "s"(tag), [w] "n"(W), LOOK_IN : "memory", "scc", "vcc")
template <int N, int W> __device__ __forceinline__ void take(f32x4& a, f32x4& b, f32x4& c, unsigned tag, rsrc_t r, int vo, int step) {
  unsigned d, spin = 1u << 18;
  if (N == 4) TAKE(FIRST(100, 102) ALSO(104) ALSO(106) ALSO(108) ALSO(110), LOOK4);
  else TAKE(FIRST(100, 104) ALSO(108), LOOK2);
}
constexpr int tofs(int n) { return n / 4 * SPB + CL * CH + (n & 3) * 4096; }
template <int I> __device__ __forceinline__ void fetchw(unsigned a) {
  asm volatile("ds_read_b128 v[%c1:%c2], %0 offset:%c3" :: "v"(a), "n"(140 + I * 16), "n"(143 + I * 16), "n"((16 + I) * 256) : "memory", "v191", "a47");
}
#define AND4(D, S) "v_and_b32 v[%c4+" #D "], v[%c4+" #S "], %0\n\tv_and_b32 v[%c4+" #D "+1], v[%c4+" #S "], %1\n\t" \
                   "v_and_b32 v[%c4+" #D "+2], v[%c4+" #S "], %2\n\tv_and_b32 v[%c4+" #D "+3], v[%c4+" #S "], %3\n\t"
template <int I> __device__ __forceinline__ void spread(u32x4 m) {
  asm volatile(AND4(0, 12) AND4(4, 13) AND4(8, 14) AND4(12, 15) :: "v"(m[0]), "v"(m[1]), "v"(m[2]), "v"(m[3]), "n"(128 + I * 16) : "memory");
}
template <int N> __device__ __forceinline__ void tail_load(rsrc_t r, int voff) {
  asm volatile("buffer_load_dwordx4 a[%c3:%c3+3], %0, %1, %2 offen nt" :: "v"(voff), "s"(r), "s"(tofs(N)), "n"(N * 4 - 16) : "memory");
  __builtin_amdgcn_sched_barrier(0);
}
template <int N, int W> __device__ __forceinline__ void tail_mul(f32x4& acc) {
  constexpr int x = N < 8 ? 64 + (N ^ 4) * 4 : N * 4 - 16, y = 128 + N * 4;
  if (N & 8) asm volatile("s_waitcnt vmcnt(%c3)\n\tv_mfma_f32_16x16x32_bf16 %0, v[%c1:%c1+3], a[%c2:%c2+3], %0" : "+a"(acc) : "n"(y), "n"(x), "n"(W) : "memory");
  else asm volatile("s_waitcnt vmcnt(%c3)\n\tv_mfma_f32_16x16x32_bf16 %0, v[%c1:%c1+3], v[%c2:%c2+3], %0" : "+a"(acc) : "n"(y), "n"(x), "n"(W) : "memory");
  __builtin_amdgcn_sched_barrier(0);
}

constexpr int LDS_T = 0;
constexpr int LDS_O = WAVES * NST * 64;
constexpr int LDS_M = LDS_O + WAVES * 256;
constexpr int LDS_N = 21 * 1024;
template <int I> using IC = std::integral_constant<int, I>;

__global__ void __launch_bounds__(NTHR)
gqa_decode_kernel(const u32x4* __restrict__ q, const unsigned char* kc, const unsigned char* vc,
                  unsigned short* __restrict__ out, unsigned char* __restrict__ ws) {
#pragma clang fp contract(off)
  __shared__ __attribute__((aligned(16))) float lds[LDS_N];
  unsigned long long stampv[STAMP_N] = {};
  STAMP(0);
  const int tid = threadIdx.x, lane = tid & 63;
  const int wave = __builtin_amdgcn_readfirstlane(tid >> 6);
  const int lin = (blockIdx.x & 7) * (NGRP * NSPLIT / 8) + (blockIdx.x >> 3);
  const int grp = lin / NSPLIT, split = lin % NSPLIT;
  const int c = lane & 15, hl = (lane >> 1) & 1;
  const bool b3 = lane & 8, b2 = lane & 4, odd = lane & 1;

  int voff = tid * 16, qo = hl * 256 + c * 16, qx = qo ^ 256;
  int to = (LDS_T + wave * NST * 64 + lane) * 4, so = (LDS_O + wave * 256 + c * 8 + (lane >> 4) * 2) * 4;
  int soff = wave * SW + (odd ? (lane >> 3) * 16 : 128 + hl * 16);
  int woff = (lane & (odd ? 2 : 64 - (SW - 128) / 4)) * SREC + soff + (lane & 4) * 2 + (odd ? 0 : (lane >> 3) * 32) + ((split - 1) & 3) * SS;
  unsigned keep = odd ? 0xffff0000u : 0x0000ffffu, flip = odd ? 0x01000c0cu : 0x0c0c0302u;
  int c0 = min((int)(blockIdx.x >> 3) * 4 + wave, CODE_LINES - 1) * 64;
  asm volatile("" : "+v"(voff), "+v"(qo), "+v"(qx), "+v"(to), "+v"(so), "+v"(soff), "+v"(woff), "+v"(keep), "+v"(flip), "+s"(c0));
  __builtin_amdgcn_sched_barrier(0);

  const unsigned char* kp = kc + (size_t)lin * SPB;
  const unsigned char* vp = vc + (size_t)lin * SPB;
  u32x4 qa, qb;
  asm volatile("buffer_load_dwordx4 %0, %2, %4, 0 offen nt\n\tbuffer_load_dwordx4 %1, %3, %4, 0 offen nt"
               : "=&v"(qa), "=&v"(qb) : "v"(qo), "v"(qx), "s"(mkrsrc(q + grp * 32, 512)) : "memory");
  auto each = [](auto n, auto f) { [&]<int... U>(std::integer_sequence<int, U...>) { (f(IC<U>()), ...); }(std::make_integer_sequence<int, decltype(n)::value>()); };
  {
    const rsrc_t r = mkrsrc(kp, 2 * CH);
    each(IC<8>(), [&](auto u) { ldstream<u>(r, voff, u * 4096); });
  }
  unsigned* ep = reinterpret_cast<unsigned*>(ws + (size_t)grp * 128);
  rsrc_t rs, ro;
  unsigned tag, tgt;
  {
    unsigned long long t0, t1;
    asm volatile("s_memrealtime %1\n\t"
                 "s_load_dword %0, %2, 0\n\t"
                 "s_load_dword s96, %3, 0\n\t"
                 "s_getpc_b64 s[94:95]\n\t"
                 "s_load_dword s97, s[94:95], %4"
                 : "=&s"(tag), "=&s"(t0) : "s"(ep), "s"(vp), "s"(c0) : "memory", "s94", "s95", "s96", "s97");
    __builtin_amdgcn_sched_barrier(0);
    rs = mkrsrc(ws + WS_S + grp * SREC, SREC);
    ro = mkrsrc(ws + WS_O + grp * OREC, OREC);
    asm volatile("s_waitcnt vmcnt(7)\n\t"
                 "s_memrealtime %2\n\t"
                 "s_waitcnt lgkmcnt(0)\n\t"
                 "s_add_u32 %0, %0, 0x9e3779b9"
                 : "+s"(tag), "+s"(t0), "=&s"(t1), "+v"(qa), "+v"(qb), "+s"(rs), "+s"(ro)
                 :: "memory", "scc", "s96", "s97");
    stampv[1] = t0; stampv[2] = t1;
    tgt = ((unsigned)t0 + LEAD) << 8;
    const unsigned t = (((unsigned)t1 + FIRST_DATA) << 8) - TAU;
    if ((int)(t - tgt) > 0) tgt = t;
    __builtin_amdgcn_sched_barrier(0);
  }
  __builtin_assume((so & 7) == 0);
  float* sT = reinterpret_cast<float*>(reinterpret_cast<char*>(lds) + to);
  float mx = -INFINITY;
#pragma unroll 1
  for (int it = 0; it < NCH / 2; ++it) {
    const rsrc_t r = mkrsrc(it + 1 < NCH / 2 ? kp + (size_t)(it + 1) * 2 * CH : vp, 2 * CH);
    float tt[2];
    each(IC<2>(), [&](auto a) {
      float z[4];
      each(IC<4>(), [&](auto b) {
        constexpr int u = a * 4 + b;
        float da = 0.f, db = 0.f;
        const u32x4 kv = Landed<u, 7>::go();
#pragma unroll
        for (int k = 0; k < 4; ++k) { da = dot2(kv[k], qa[k], da); db = dot2(kv[k], qb[k], db); }
        asm volatile("" : "+v"(da), "+v"(db));
        __builtin_amdgcn_sched_barrier(0);
        if (b == 0) pace(tgt, TAU);
        ldstream<u>(r, voff, u * 4096);
        z[b] = da + dpp<XOR2>(db);
      });
      const float u0 = z[0] + dpp<ROR8>(z[0]), u2 = z[2] + dpp<ROR8>(z[2]);
      const float u1 = z[1] + dpp<ROR8>(z[1]), u3 = z[3] + dpp<ROR8>(z[3]);
      const float w0 = b3 ? u2 : u0, w1 = b3 ? u3 : u1;
      const float x0 = w0 + dpp<0x104>(w0);
      const float x1 = w1 + dpp<0x114>(w1);
      tt[a] = b2 ? x1 : x0;
      asm volatile("" : "+v"(tt[a]));
      __builtin_amdgcn_sched_barrier(0);
    });
    const float t0 = tt[0], t1 = tt[1];
    const float t = (odd ? t1 : t0) + dpp<XOR1>(odd ? t0 : t1);
    mx = fmaxf(mx, t);
    sT[it * 64] = t;
    __builtin_amdgcn_sched_barrier(0);
  }
  STAMP(3);
  float sc[8];
#pragma unroll
  for (int i = 0; i < 8; ++i) sc[i] = sT[i * 64];
  __builtin_amdgcn_sched_barrier(0);
  float mc = head_max(mx) * C2;
  auto pack = [](float a, float b) { bf16x2 v; v[0] = (__bf16)a; v[1] = (__bf16)b; return __builtin_bit_cast(unsigned, v); };
  auto wexp = [&](float t) { return __builtin_amdgcn_exp2f(fmaf(t, C2, -mc)); };
  auto both = [&](float p) { return pack(p, dpp<XOR1>(p)); };
  auto same = [&](unsigned y) { return y & keep; };
  auto swap = [&](unsigned y) { return __builtin_amdgcn_perm(y, y, flip); };
  unsigned* sW = reinterpret_cast<unsigned*>(sT) + 8 * 64;
  float l = 0.f;
  {
#pragma unroll
    for (int i = 0; i < 7; ++i) { const float p = wexp(sc[i]); l += p; sW[i * 64] = both(p); }
    const float p = wexp(sc[7]);
    sW[7 * 64] = both(p);
    const unsigned w = pack(p, dpp<XOR2>(p));
    l = head_sum(l + p);
    const u32x2 rec = {tag, odd ? w : __float_as_uint(b2 ? l : mc)};
    asm volatile("buffer_store_dwordx2 %0, %1, %2, 0 offen sc1" :: "v"(rec), "v"(woff), "s"(rs) : "memory");
    __builtin_amdgcn_sched_barrier(0);
  }
  f32x4 acc = {0.f, 0.f, 0.f, 0.f};
  u32x4 A0 = {0u, 0u, 0u, 0u}, A1 = {0u, 0u, 0u, 0u};
  auto chunk = [&](auto h, auto w, auto next, unsigned P, rsrc_t r, int so, unsigned dt) {
    each(IC<4>(), [&](auto b) {
      constexpr int u = h * 4 + b;
      u32x4& A = (b & 1) ? A1 : A0;
      place4<b>(A, P);
      asm volatile("" : "+v"(A));
      acc = mfma(A, Landed<u, next ? w : w - b>::go(), acc);
      if (next && b == 0) pace(tgt, dt);
      if (next) ldstream<u>(r, voff, so + b * 4096);
    });
  };
  auto pair = [&](int g, auto w) {
    const rsrc_t r = mkrsrc(vp + (size_t)(g + 2) * CH, 2 * CH);
    const unsigned y = sW[g * 32];
    chunk(IC<0>(), w, IC<1>(), same(y), r, 0, TAU);
    chunk(IC<1>(), w, IC<1>(), swap(y), r, CH, TAU);
  };
  auto unload = [&] {
    *reinterpret_cast<f32x2*>((char*)lds + so) = f32x2{acc[0], acc[1]};
    *reinterpret_cast<f32x2*>((char*)lds + so + 512) = f32x2{acc[2], acc[3]};
    __builtin_amdgcn_sched_barrier(0);
  };
  auto sync = [] { asm volatile("s_waitcnt lgkmcnt(0)\n\ts_barrier" ::: "memory"); };
  float* sM = lds + LDS_M + hl * 4 + wave;
  auto row = [&](int i) { return *reinterpret_cast<const f32x4*>(lds + LDS_M + i * 8 + (tid >> 7) * 4); };
  auto max4 = [](f32x4 m) { return fmaxf(fmaxf(m[0], m[1]), fmaxf(m[2], m[3])); };
  f32x4 fw;
  pair(0, IC<8>());
#pragma unroll 1
  for (int g = 2; g < 10; g += 2) pair(g, IC<7>());
  const rsrc_t rg = mkrsrc(vc + (size_t)grp * (NSPLIT * SPB), NSPLIT * SPB);

  if (split) {
    if ((lane & 61) == 0) sM[0] = mc;
    pair(10, IC<7>());
    const unsigned y6 = sW[6 * 64];
    unsigned P1 = swap(y6), P2 = same(sW[7 * 64]);
    chunk(IC<0>(), IC<7>(), IC<1>(), same(y6), rg, split * SPB + 14 * CH, TAU);
    sync();
    {
      const f32x4 tm = row(0);
      const float M = max4(tm);
#pragma unroll
      for (int w = 0; w < WAVES; ++w) fw[w] = __builtin_amdgcn_exp2f(tm[w] - M);
      asm volatile("" : "+v"(fw));
    }
    int po = lane < 32 ? wave * 4096 + lane * 128 : 1 << 21;
    int oo = tid & 1 ? OREC : (split - 1) * OS + tid * 8;
    asm volatile("" : "+v"(P1), "+v"(P2), "+v"(po), "+v"(oo), "+s"(ro));
    pace(tgt, TAU);
    pull(rg, po, split * SPB + CL * CH);
    STAMP(5);
    chunk(IC<1>(), IC<8>(), IC<0>(), P1, rg, 0, 0);
    chunk(IC<0>(), IC<4>(), IC<0>(), P2, rg, 0, 0);
    unload();
    sync();
    float on = fw[0] * lds[LDS_O + tid];
#pragma unroll
    for (int w = 1; w < WAVES; ++w) on = fmaf(fw[w], lds[LDS_O + w * 256 + tid], on);
    post(tag, on, dpp<XOR1>(on), ro, oo);
    STAMP(6);
    asm volatile("s_waitcnt vmcnt(0)" ::: "memory");
    STAMP_FLUSH();
    return;
  }

  look<4>(rs, soff, SS);
  pair(10, IC<10>());
  unsigned P0, P1, P2;
  {
    f32x4 x[3];
    take<4, 8>(x[0], x[1], x[2], tag, rs, soff, SS);
    constexpr int EVEN = 0xA0, ODD = 0xF5;
    float ms[3], m2 = mc;
#pragma unroll
    for (int s = 0; s < 3; ++s) { ms[s] = dpp<EVEN>(x[s][1]); m2 = fmaxf(m2, ms[s]); }
    const float f = __builtin_amdgcn_exp2f(mc - m2), fo = dpp<XOR2>(f);
    const float f0 = hl ? fo : f, f1 = hl ? f : fo;
    mc = m2; l *= f;
    acc[0] *= f0; acc[1] *= f0; acc[2] *= f1; acc[3] *= f1;
    asm volatile("" : "+a"(acc));
    const unsigned y6 = both(wexp(sc[6])), y7 = both(wexp(sc[7]));
    P0 = same(y6); P1 = swap(y6); P2 = same(y7);
    unsigned* sX = sW + 8 * 64 - (lane & 15) + (lane & 3) * 4 + ((lane >> 2) & 3);
    sX[0] = swap(y7);
#pragma unroll
    for (int s = 0; s < 3; ++s) {
      const float fs = __builtin_amdgcn_exp2f(ms[s] - m2);
      const float h0 = x[s][1], h1 = x[s][3];
      const unsigned pp = __float_as_uint(dpp<ODD>(b2 ? h1 : h0));
      const float p = __uint_as_float(hl ? pp & 0xffff0000u : pp << 16) * fs;
      sX[(1 + s) * 64] = same(pack(p, p));
      l = fmaf(h1, fs, l);
    }
    if ((lane & 61) == 0) { sM[0] = mc; sM[8] = l; sM[16] = ms[0]; sM[24] = ms[1]; sM[32] = ms[2]; }
    each(IC<4>(), [&](auto i) { fetchw<i>(to - c * 4 + (lane & 3) * 16); });
  }
  chunk(IC<0>(), IC<7>(), IC<1>(), P0, rg, 14 * CH, TAU);
  {
    u32x4 bm;
#pragma unroll
    for (int i = 0; i < 4; ++i) bm[i] = ((lane >> 2) & 3) == i ? ~0u : 0u;
    each(IC<4>(), [&](auto i) { spread<i>(bm); });
  }
  chunk(IC<1>(), IC<7>(), IC<1>(), P1, rg, tofs(0), TAU);
  STAMP(7);
  sync();
  if (lane == 0) asm volatile("global_store_dword %0, %1, %2 sc1" :: "v"(0), "v"(tag), "s"(ep) : "memory");
  float cs[3];
  int oo = tid * 8;
  {
    const f32x4 tm = row(0), tl = row(1);
    const float M = max4(tm);
    float L = 0.f;
#pragma unroll
    for (int w = 0; w < WAVES; ++w) { fw[w] = __builtin_amdgcn_exp2f(tm[w] - M); L = fmaf(fw[w], tl[w], L); }
    const float r = __builtin_amdgcn_rcpf(L);
    fw *= r;
#pragma unroll
    for (int s = 0; s < 3; ++s) cs[s] = __builtin_amdgcn_exp2f(max4(row(2 + s)) - M) * r;
    asm volatile("" : "+v"(fw), "+v"(cs[0]), "+v"(cs[1]), "+v"(cs[2]), "+v"(oo), "+s"(ro));
  }
  chunk(IC<0>(), IC<8>(), IC<1>(), P2, rg, tofs(4), DEL);
  each(IC<8>(), [&](auto u) { tail_load<8 + u>(rg, voff); });
  each(IC<8>(), [&](auto k) { tail_mul<k, 15 - k + (k < 4)>(acc); });
  look<2>(ro, oo, OS);
  each(IC<8>(), [&](auto k) { tail_mul<8 + k, 10 - k>(acc); });
  asm volatile("s_nop 7\n\ts_nop 1" : "+a"(acc));
  unload();
  float y = 0.f;
  {
    f32x4 x[3];
    take<2, 0>(x[0], x[1], x[2], tag, ro, oo, OS);
#pragma unroll
    for (int s = 0; s < 3; ++s) y = fmaf(cs[s], x[s][1], y);
    asm volatile("" : "+v"(y));
  }
  sync();
#pragma unroll
  for (int w = 0; w < WAVES; ++w) y = fmaf(fw[w], lds[LDS_O + w * 256 + tid], y);
  out[grp * 256 + tid] = __builtin_bit_cast(unsigned short, (__bf16)y);
  STAMP(4); STAMP_FLUSH();
}

#define K_OFF 0
#define K_REF 10
__device__ unsigned stat_one(const unsigned long long* sp, unsigned ref, int code, unsigned* sh) {
  const int ph = code % 10, kind = code / 10;
  unsigned mx = 0, mn = ~0u, sm = 0, n = 0;
  for (int i = threadIdx.x; i < STAMP_NW; i += 256) {
    unsigned long long braw = sp[8 * i + ph]; unsigned b = (unsigned)braw - ref;
    if (braw) { mx = max(mx, b); mn = min(mn, b); sm += b; ++n; }
  }
  if (kind == 3 || kind == 4) {
    unsigned s2 = sm, n2 = n;
    s2 += __shfl_xor(s2, 1); n2 += __shfl_xor(n2, 1); s2 += __shfl_xor(s2, 2); n2 += __shfl_xor(n2, 2); s2 += __shfl_xor(s2, 32); n2 += __shfl_xor(n2, 32);
    __shared__ unsigned cs[64];
    __syncthreads();
    if ((threadIdx.x & 35) == 0) { int c = (threadIdx.x >> 2) & 7, w = threadIdx.x >> 6; cs[w * 8 + c] = s2; cs[32 + w * 8 + c] = n2; }
    __syncthreads();
    unsigned best = kind == 3 ? 0u : ~0u;
    for (int c = 0; c < 8; ++c) { unsigned m = (cs[c] + cs[8 + c] + cs[16 + c] + cs[24 + c]) / (cs[32 + c] + cs[40 + c] + cs[48 + c] + cs[56 + c]); best = kind == 3 ? max(best, m) : min(best, m); }
    return best;
  }
  for (int o = 32; o; o >>= 1) { mx = max(mx, (unsigned)__shfl_xor(mx, o)); mn = min(mn, (unsigned)__shfl_xor(mn, o)); sm += __shfl_xor(sm, o); n += __shfl_xor(n, o); }
  __syncthreads();
  if ((threadIdx.x & 63) == 0) { int w = threadIdx.x >> 6; sh[w] = mx; sh[4 + w] = mn; sh[8 + w] = sm; sh[12 + w] = n; }
  __syncthreads();
  mx = max(max(sh[0], sh[1]), max(sh[2], sh[3])); mn = min(min(sh[4], sh[5]), min(sh[6], sh[7]));
  sm = sh[8] + sh[9] + sh[10] + sh[11]; n = sh[12] + sh[13] + sh[14] + sh[15];
  return kind == 0 ? mx : kind == 1 ? mn : sm / n;
}
__global__ void gqa_stat_kernel(unsigned char* ws) {
  const unsigned long long* sp = (const unsigned long long*)(ws + STAMP_OFF);
  const unsigned ref = (unsigned)sp[0] - 100000u;
  __shared__ unsigned sh[16];
  const unsigned a = stat_one(sp, ref, K_STAT < 0 ? 0 : K_STAT, sh), b = stat_one(sp, ref, K_REF, sh);
  unsigned dl = K_STAT < 0 ? ((a - b) >> 20) : K_SCALE * (a - b - K_OFF);
  if (dl > 100000) dl = 100000;
  const unsigned long long t0 = __builtin_amdgcn_s_memrealtime();
  while (__builtin_amdgcn_s_memrealtime() - t0 < dl) __builtin_amdgcn_s_sleep(2);
}
extern "C" void kernel_launch(void* const* d_in, const int* in_sizes, int n_in,
                              void* d_out, int out_size, void* d_ws, size_t ws_size,
                              hipStream_t stream) {
  (void)in_sizes; (void)n_in; (void)out_size; (void)ws_size;
  gqa_decode_kernel<<<NGRP * NSPLIT, NTHR, 0, stream>>>((const u32x4*)d_in[0], (const unsigned char*)d_in[1], (const unsigned char*)d_in[2],
                                                        (unsigned short*)d_out, (unsigned char*)d_ws);
  gqa_stat_kernel<<<1, 256, 0, stream>>>((unsigned char*)d_ws);
}
```

```cpp
#include <hip/hip_runtime.h>
#include <hip/hip_bf16.h>
#include <stdint.h>
#include <type_traits>
#include <utility>

typedef __attribute__((ext_vector_type(4))) unsigned u32x4;
typedef __attribute__((ext_vector_type(2))) unsigned u32x2;
typedef __attribute__((ext_vector_type(4))) float f32x4;
typedef __attribute__((ext_vector_type(2))) float f32x2;
typedef __attribute__((ext_vector_type(8))) __bf16 bf16x8;
typedef __attribute__((ext_vector_type(2))) __bf16 bf16x2;
typedef __attribute__((ext_vector_type(4))) int rsrc_t;

constexpr int NB = 4, HKV = 16, SK = 4096, D = 128;
constexpr int NGRP = NB * HKV;
constexpr int NSPLIT = 4, WAVES = 4, NTHR = WAVES * 64;
constexpr int KPS = SK / NSPLIT;
constexpr int SPB = KPS * D * 2;
constexpr int CH = 16384;
constexpr int NCH = SPB / CH;
constexpr int CL = NCH - 1;
constexpr int NST = 24;
constexpr float C2 = 0.08838834764831845f * 1.4426950408889634f;

constexpr unsigned TAU = (unsigned)(NGRP * NSPLIT * 16384.0 / 7700e9 * 1e8 * 256);
constexpr unsigned LEAD = 25, FIRST_DATA = 5;
constexpr unsigned DEL = 30 << 8;
constexpr int CODE_LINES = 128;

constexpr size_t WS_S = 512u << 10, WS_O = 1u << 20;
constexpr int SW = 192, SS = WAVES * SW, SREC = 3 * SS, OS = 2048, OREC = 3 * OS;

__device__ __forceinline__ float dot2(unsigned a, unsigned b, float acc) {
  return __builtin_amdgcn_fdot2_f32_bf16(__builtin_bit_cast(bf16x2, a), __builtin_bit_cast(bf16x2, b), acc, false);
}
template <int CTRL> __device__ __forceinline__ float dpp(float x) {
  return __builtin_bit_cast(float, __builtin_amdgcn_mov_dpp(__builtin_bit_cast(int, x), CTRL, 0xf, 0xf, true));
}
constexpr int XOR1 = 0xB1, XOR2 = 0x4E, ROR4 = 0x124, ROR8 = 0x128, ROR12 = 0x12C, IDENT = 0xE4;
#define STEP(OP, HOW) "s_nop 1\n\t" OP "_dpp %0, %0, %0 " HOW " row_mask:0xf bank_mask:0xf\n\t"
#define SWAP(OP, N) "v_mov_b32 %1, %0\n\ts_nop 1\n\tv_permlane" N "_swap_b32 %0, %1\n\t" OP " %0, %0, %1\n\t"
#define ACROSS(NAME, OP) \
  __device__ __forceinline__ float NAME(float x) { \
    float y; \
    asm(STEP(OP, "quad_perm:[1,0,3,2]") STEP(OP, "row_ror:4") STEP(OP, "row_ror:8") SWAP(OP, "16") SWAP(OP, "32") : "+v"(x), "=&v"(y)); \
    return x; }
ACROSS(head_max, "v_max_f32") ACROSS(head_sum, "v_add_f32")
__device__ __forceinline__ rsrc_t mkrsrc(const void* p, int bytes) {
  const unsigned long long a = (unsigned long long)p;
  return rsrc_t{(int)(unsigned)a, (int)(unsigned)(a >> 32) & 0xffff, bytes, 0x00020000};
}
template <int U> __device__ __forceinline__ void ldstream(rsrc_t r, int voff, int soff);
template <int U, int N> struct Landed;
#define RING(U, A, B, C, D) \
  template <> __device__ __forceinline__ void ldstream<U>(rsrc_t r, int voff, int soff) { \
    asm volatile("buffer_load_dwordx4 v[" #A ":" #D "], %0, %1, %2 offen nt" :: "v"(voff), "s"(r), "s"(soff) : "memory", "v" #A, "v" #B, "v" #C, "v" #D); \
    __builtin_amdgcn_sched_barrier(0); } \
  template <int N> struct Landed<U, N> { static __device__ __forceinline__ u32x4 go() { \
    u32x4 v; \
    asm volatile("s_waitcnt vmcnt(%1)" : "={v[" #A ":" #D "]}"(v) : "n"(N) : "memory"); \
    __builtin_amdgcn_sched_barrier(0); \
    return v; } };
RING(0, 64, 65, 66, 67) RING(1, 68, 69, 70, 71) RING(2, 72, 73, 74, 75) RING(3, 76, 77, 78, 79)
RING(4, 80, 81, 82, 83) RING(5, 84, 85, 86, 87) RING(6, 88, 89, 90, 91) RING(7, 92, 93, 94, 95)
__device__ __forceinline__ void pace(unsigned& tgt, unsigned dt) {
  asm volatile("s_add_u32 %0, %0, %1\n\t"
               "s_branch 1f\n"
               "0:\n\t"
               "s_sleep 1\n"
               "1:\n\t"
               "s_memrealtime s[96:97]\n\t"
               "s_waitcnt lgkmcnt(0)\n\t"
               "s_lshl_b32 s96, s96, 8\n\t"
               "s_sub_u32 s96, s96, %0\n\t"
               "s_cmp_lt_i32 s96, 0\n\t"
               "s_cbranch_scc1 0b"
               : "+s"(tgt) : "s"(dt) : "s96", "s97", "scc", "memory");
  __builtin_amdgcn_sched_barrier(0);
}
template <int R, int B> __device__ __forceinline__ unsigned place(unsigned old, unsigned src) {
  constexpr int n = (R - B) & 3;
  constexpr int ctrl = n == 0 ? IDENT : n == 1 ? ROR4 : n == 2 ? ROR8 : ROR12;
  return (unsigned)__builtin_amdgcn_update_dpp((int)old, (int)src, ctrl, 0xf, 1 << R, false);
}
template <int B> __device__ __forceinline__ void place4(u32x4& a, unsigned src) {
  a[0] = place<0, B>(a[0], src); a[1] = place<1, B>(a[1], src); a[2] = place<2, B>(a[2], src); a[3] = place<3, B>(a[3], src);
}
__device__ __forceinline__ f32x4 mfma(u32x4 a, u32x4 b, f32x4 c) {
  c = __builtin_amdgcn_mfma_f32_16x16x32_bf16(__builtin_bit_cast(bf16x8, a), __builtin_bit_cast(bf16x8, b), c, 0, 0, 0);
  asm volatile("" : "+a"(c));
  __builtin_amdgcn_sched_barrier(0);
  return c;
}
#define LOOK(N, A, B, C) "buffer_load_dwordx" #N " v[100:" #A "], %[vo], %[rp], 0 offen sc1\n\t" \
                         "buffer_load_dwordx" #N " v[104:" #B "], %[vo], %[rp], %[s2] offen sc1\n\t" \
                         "buffer_load_dwordx" #N " v[108:" #C "], %[vo], %[rp], %[s3] offen sc1\n\t"
#define LOOK4 LOOK(4, 103, 107, 111)
#define LOOK2 LOOK(2, 101, 105, 109)
#define LOOK_IN [vo] "v"(vo), [rp] "s"(r), [s2] "s"(step), [s3] "s"(2 * step)
#define LOOK_REGS "v100", "v101", "v102", "v103", "v104", "v105", "v106", "v107", "v108", "v109", "v110", "v111"
template <int N> __device__ __forceinline__ void look(rsrc_t r, int vo, int step) {
  if (N == 4) asm volatile(LOOK4 :: LOOK_IN : "memory", LOOK_REGS);
  else asm volatile(LOOK2 :: LOOK_IN : "memory", LOOK_REGS);
  __builtin_amdgcn_sched_barrier(0);
}
__device__ __forceinline__ void pull(rsrc_t r, int vo, int so) {
  asm volatile("buffer_load_dword v100, %0, %1, %2 offen sc0 sc1" :: "v"(vo), "s"(r), "s"(so) : "memory", "v100");
  __builtin_amdgcn_sched_barrier(0);
}
__device__ __forceinline__ void post(unsigned tag, float a, float b, rsrc_t r, int vo) {
  const u32x4 rec = {tag, __float_as_uint(a), tag, __float_as_uint(b)};
  asm volatile("buffer_store_dwordx4 %0, %1, %2, 0 offen sc1\n\ts_nop 1" :: "v"(rec), "v"(vo), "s"(r) : "memory");
}
#define FIRST(A, B) "v_bitop3_b32 %[d], v" #A ", v" #B ", %[tag] bitop3:0x7e\n\t"
#define ALSO(A) "v_bitop3_b32 %[d], %[d], v" #A ", %[tag] bitop3:0xf6\n\t"
#define TAKE(CHECK, AGAIN) \
  asm volatile("s_waitcnt vmcnt(%[w])\n" \
               "0:\n\t" CHECK \
               "v_cmp_ne_u32 vcc, 0, %[d]\n\t" \
               "s_cbranch_vccz 1f\n\t" \
               "s_sub_u32 %[n], %[n], 1\n\t" \
               "s_cbranch_scc1 1f\n\t" AGAIN \
               "s_waitcnt vmcnt(0)\n\t" \
               "s_branch 0b\n" \
               "1:" \
               : "=&{v[100:103]}"(a), "=&{v[104:107]}"(b), "=&{v[108:111]}"(c), [d] "=&v"(d), [n] "+s"(spin) \
               : [tag] "s"(tag), [w] "n"(W), LOOK_IN : "memory", "scc", "vcc")
template <int N, int W> __device__ __forceinline__ void take(f32x4& a, f32x4& b, f32x4& c, unsigned tag, rsrc_t r, int vo, int step) {
  unsigned d, spin = 1u << 18;
  if (N == 4) TAKE(FIRST(100, 102) ALSO(104) ALSO(106) ALSO(108) ALSO(110), LOOK4);
  else TAKE(FIRST(100, 104) ALSO(108), LOOK2);
}
constexpr int tofs(int n) { return n / 4 * SPB + CL * CH + (n & 3) * 4096; }
template <int I> __device__ __forceinline__ void fetchw(unsigned a) {
  asm volatile("ds_read_b128 v[%c1:%c2], %0 offset:%c3" :: "v"(a), "n"(140 + I * 16), "n"(143 + I * 16), "n"((16 + I) * 256) : "memory", "v191", "a47");
}
#define AND4(D, S) "v_and_b32 v[%c4+" #D "], v[%c4+" #S "], %0\n\tv_and_b32 v[%c4+" #D "+1], v[%c4+" #S "], %1\n\t" \
                   "v_and_b32 v[%c4+" #D "+2], v[%c4+" #S "], %2\n\tv_and_b32 v[%c4+" #D "+3], v[%c4+" #S "], %3\n\t"
template <int I> __device__ __forceinline__ void spread(u32x4 m) {
  asm volatile(AND4(0, 12) AND4(4, 13) AND4(8, 14) AND4(12, 15) :: "v"(m[0]), "v"(m[1]), "v"(m[2]), "v"(m[3]), "n"(128 + I * 16) : "memory");
}
template <int N> __device__ __forceinline__ void tail_load(rsrc_t r, int voff) {
  asm volatile("buffer_load_dwordx4 a[%c3:%c3+3], %0, %1, %2 offen nt" :: "v"(voff), "s"(r), "s"(tofs(N)), "n"(N * 4 - 16) : "memory");
  __builtin_amdgcn_sched_barrier(0);
}
template <int N, int W> __device__ __forceinline__ void tail_mul(f32x4& acc) {
  constexpr int x = N < 8 ? 64 + (N ^ 4) * 4 : N * 4 - 16, y = 128 + N * 4;
  if (N & 8) asm volatile("s_waitcnt vmcnt(%c3)\n\tv_mfma_f32_16x16x32_bf16 %0, v[%c1:%c1+3], a[%c2:%c2+3], %0" : "+a"(acc) : "n"(y), "n"(x), "n"(W) : "memory");
  else asm volatile("s_waitcnt vmcnt(%c3)\n\tv_mfma_f32_16x16x32_bf16 %0, v[%c1:%c1+3], v[%c2:%c2+3], %0" : "+a"(acc) : "n"(y), "n"(x), "n"(W) : "memory");
  __builtin_amdgcn_sched_barrier(0);
}

constexpr int LDS_T = 0;
constexpr int LDS_O = WAVES * NST * 64;
constexpr int LDS_M = LDS_O + WAVES * 256;
constexpr int LDS_N = 21 * 1024;
template <int I> using IC = std::integral_constant<int, I>;

__global__ void __launch_bounds__(NTHR)
gqa_decode_kernel(const u32x4*, const unsigned char*, const unsigned char*, unsigned char*, unsigned short*) {
#pragma clang fp contract(off)
  __shared__ __attribute__((aligned(16))) float lds[LDS_N];
  typedef __attribute__((ext_vector_type(4))) unsigned long long u64x4;
  u64x4 ka;
  asm volatile("s_load_dwordx8 %0, %1, 0" : "=s"(ka) : "s"(__builtin_amdgcn_kernarg_segment_ptr()) : "memory");
  __builtin_amdgcn_sched_barrier(0);
  const int tid = threadIdx.x, lane = tid & 63;
  const int wave = __builtin_amdgcn_readfirstlane(tid >> 6);
  const int lin = (blockIdx.x & 7) * (NGRP * NSPLIT / 8) + (blockIdx.x >> 3);
  const int grp = lin / NSPLIT, split = lin % NSPLIT;
  const int c = lane & 15, hl = (lane >> 1) & 1;
  const bool b3 = lane & 8, b2 = lane & 4, odd = lane & 1;

  int voff = tid * 16, qo = hl * 256 + c * 16, qx = qo ^ 256;
  int to = (LDS_T + wave * NST * 64 + lane) * 4, so = (LDS_O + wave * 256 + c * 8 + (lane >> 4) * 2) * 4;
  int soff = wave * SW + (odd ? (lane >> 3) * 16 : 128 + hl * 16);
  int woff = (lane & (odd ? 2 : 64 - (SW - 128) / 4)) * SREC + soff + (lane & 4) * 2 + (odd ? 0 : (lane >> 3) * 32) + ((split - 1) & 3) * SS;
  unsigned keep = odd ? 0xffff0000u : 0x0000ffffu, flip = odd ? 0x01000c0cu : 0x0c0c0302u;
  int c0 = min((int)(blockIdx.x >> 3) * 4 + wave, CODE_LINES - 1) * 64;
  unsigned long long lo = (unsigned long long)lin * SPB; unsigned qg = grp * 512;
  asm volatile("s_waitcnt lgkmcnt(0)" : "+v"(voff), "+v"(qo), "+v"(qx), "+v"(to), "+v"(so), "+v"(soff), "+v"(woff), "+v"(keep), "+v"(flip), "+s"(c0), "+s"(ka), "+s"(lo), "+s"(qg) :: "memory");
  __builtin_amdgcn_sched_barrier(0);
  const unsigned char* q = (const unsigned char*)ka[0];
  const unsigned char* kc = (const unsigned char*)ka[1];
  const unsigned char* vc = (const unsigned char*)ka[2];
  unsigned char* ws = (unsigned char*)ka[3];

  const unsigned char* kp = kc + lo;
  const unsigned char* vp = vc + lo;
  u32x4 qa, qb;
  asm volatile("buffer_load_dwordx4 %0, %2, %4, 0 offen nt\n\tbuffer_load_dwordx4 %1, %3, %4, 0 offen nt"
               : "=&v"(qa), "=&v"(qb) : "v"(qo), "v"(qx), "s"(mkrsrc(q + qg, 512)) : "memory");
  auto each = [](auto n, auto f) { [&]<int... U>(std::integer_sequence<int, U...>) { (f(IC<U>()), ...); }(std::make_integer_sequence<int, decltype(n)::value>()); };
  {
    const rsrc_t r = mkrsrc(kp, 2 * CH);
    each(IC<8>(), [&](auto u) { ldstream<u>(r, voff, u * 4096); });
  }
  unsigned* ep = reinterpret_cast<unsigned*>(ws + (size_t)grp * 128);
  rsrc_t rs, ro;
  unsigned tag, tgt;
  __attribute__((address_space(1))) unsigned short* out;
  {
    unsigned long long t0, t1;
    asm volatile("s_memrealtime %1\n\t"
                 "s_load_dword %0, %2, 0\n\t"
                 "s_load_dword s96, %3, 0\n\t"
                 "s_getpc_b64 s[94:95]\n\t"
                 "s_load_dword s97, s[94:95], %4\n\t"
                 "s_load_dwordx2 s[92:93], %5, 32"
                 : "=&s"(tag), "=&s"(t0) : "s"(ep), "s"(vp), "s"(c0), "s"(__builtin_amdgcn_kernarg_segment_ptr()) : "memory", "s92", "s93", "s94", "s95", "s96", "s97");
    __builtin_amdgcn_sched_barrier(0);
    rs = mkrsrc(ws + WS_S + grp * SREC, SREC);
    ro = mkrsrc(ws + WS_O + grp * OREC, OREC);
    asm volatile("s_waitcnt vmcnt(7)\n\t"
                 "s_memrealtime %2\n\t"
                 "s_waitcnt lgkmcnt(0)\n\t"
                 "s_add_u32 %0, %0, 0x9e3779b9"
                 : "+s"(tag), "+s"(t0), "=&s"(t1), "+v"(qa), "+v"(qb), "+s"(rs), "+s"(ro), "={s[92:93]}"(out)
                 :: "memory", "scc", "s96", "s97");
    tgt = ((unsigned)t0 + LEAD) << 8;
    const unsigned t = (((unsigned)t1 + FIRST_DATA) << 8) - TAU;
    if ((int)(t - tgt) > 0) tgt = t;
    __builtin_amdgcn_sched_barrier(0);
  }
  __builtin_assume((so & 7) == 0);
  float* sT = reinterpret_cast<float*>(reinterpret_cast<char*>(lds) + to);
  float mx = -INFINITY;
#pragma unroll 1
  for (int it = 0; it < NCH / 2; ++it) {
    const rsrc_t r = mkrsrc(it + 1 < NCH / 2 ? kp + (size_t)(it + 1) * 2 * CH : vp, 2 * CH);
    float tt[2];
    each(IC<2>(), [&](auto a) {
      float z[4];
      each(IC<4>(), [&](auto b) {
        constexpr int u = a * 4 + b;
        float da = 0.f, db = 0.f;
        const u32x4 kv = Landed<u, 7>::go();
#pragma unroll
        for (int k = 0; k < 4; ++k) { da = dot2(kv[k], qa[k], da); db = dot2(kv[k], qb[k], db); }
        asm volatile("" : "+v"(da), "+v"(db));
        __builtin_amdgcn_sched_barrier(0);
        if (b == 0) pace(tgt, TAU);
        ldstream<u>(r, voff, u * 4096);
        z[b] = da + dpp<XOR2>(db);
      });
      const float u0 = z[0] + dpp<ROR8>(z[0]), u2 = z[2] + dpp<ROR8>(z[2]);
      const float u1 = z[1] + dpp<ROR8>(z[1]), u3 = z[3] + dpp<ROR8>(z[3]);
      const float w0 = b3 ? u2 : u0, w1 = b3 ? u3 : u1;
      const float x0 = w0 + dpp<0x104>(w0);
      const float x1 = w1 + dpp<0x114>(w1);
      tt[a] = b2 ? x1 : x0;
      asm volatile("" : "+v"(tt[a]));
      __builtin_amdgcn_sched_barrier(0);
    });
    const float t0 = tt[0], t1 = tt[1];
    const float t = (odd ? t1 : t0) + dpp<XOR1>(odd ? t0 : t1);
    mx = fmaxf(mx, t);
    sT[it * 64] = t;
    __builtin_amdgcn_sched_barrier(0);
  }
  float sc[8];
#pragma unroll
  for (int i = 0; i < 8; ++i) sc[i] = sT[i * 64];
  __builtin_amdgcn_sched_barrier(0);
  float mc = head_max(mx) * C2;
  auto pack = [](float a, float b) { bf16x2 v; v[0] = (__bf16)a; v[1] = (__bf16)b; return __builtin_bit_cast(unsigned, v); };
  auto wexp = [&](float t) { return __builtin_amdgcn_exp2f(fmaf(t, C2, -mc)); };
  auto both = [&](float p) { return pack(p, dpp<XOR1>(p)); };
  auto same = [&](unsigned y) { return y & keep; };
  auto swap = [&](unsigned y) { return __builtin_amdgcn_perm(y, y, flip); };
  unsigned* sW = reinterpret_cast<unsigned*>(sT) + 8 * 64;
  float l = 0.f;
  {
#pragma unroll
    for (int i = 0; i < 7; ++i) { const float p = wexp(sc[i]); l += p; sW[i * 64] = both(p); }
    const float p = wexp(sc[7]);
    sW[7 * 64] = both(p);
    const unsigned w = pack(p, dpp<XOR2>(p));
    l = head_sum(l + p);
    const u32x2 rec = {tag, odd ? w : __float_as_uint(b2 ? l : mc)};
    asm volatile("buffer_store_dwordx2 %0, %1, %2, 0 offen sc1" :: "v"(rec), "v"(woff), "s"(rs) : "memory");
    __builtin_amdgcn_sched_barrier(0);
  }
  f32x4 acc = {0.f, 0.f, 0.f, 0.f};
  u32x4 A0 = {0u, 0u, 0u, 0u}, A1 = {0u, 0u, 0u, 0u};
  auto chunk = [&](auto h, auto w, auto next, unsigned P, rsrc_t r, int so, unsigned dt) {
    each(IC<4>(), [&](auto b) {
      constexpr int u = h * 4 + b;
      u32x4& A = (b & 1) ? A1 : A0;
      place4<b>(A, P);
      asm volatile("" : "+v"(A));
      acc = mfma(A, Landed<u, next ? w : w - b>::go(), acc);
      if (next && b == 0) pace(tgt, dt);
      if (next) ldstream<u>(r, voff, so + b * 4096);
    });
  };
  auto pair = [&](int g, auto w) {
    const rsrc_t r = mkrsrc(vp + (size_t)(g + 2) * CH, 2 * CH);
    const unsigned y = sW[g * 32];
    chunk(IC<0>(), w, IC<1>(), same(y), r, 0, TAU);
    chunk(IC<1>(), w, IC<1>(), swap(y), r, CH, TAU);
  };
  auto unload = [&] {
    *reinterpret_cast<f32x2*>((char*)lds + so) = f32x2{acc[0], acc[1]};
    *reinterpret_cast<f32x2*>((char*)lds + so + 512) = f32x2{acc[2], acc[3]};
    __builtin_amdgcn_sched_barrier(0);
  };
  auto sync = [] { asm volatile("s_waitcnt lgkmcnt(0)\n\ts_barrier" ::: "memory"); };
  float* sM = lds + LDS_M + hl * 4 + wave;
  auto row = [&](int i) { return *reinterpret_cast<const f32x4*>(lds + LDS_M + i * 8 + (tid >> 7) * 4); };
  auto max4 = [](f32x4 m) { return fmaxf(fmaxf(m[0], m[1]), fmaxf(m[2], m[3])); };
  f32x4 fw;
  pair(0, IC<8>());
#pragma unroll 1
  for (int g = 2; g < 10; g += 2) pair(g, IC<7>());
  const rsrc_t rg = mkrsrc(vc + (size_t)grp * (NSPLIT * SPB), NSPLIT * SPB);

  if (split) {
    if ((lane & 61) == 0) sM[0] = mc;
    pair(10, IC<7>());
    const unsigned y6 = sW[6 * 64];
    unsigned P1 = swap(y6), P2 = same(sW[7 * 64]);
    chunk(IC<0>(), IC<7>(), IC<1>(), same(y6), rg, split * SPB + 14 * CH, TAU);
    sync();
    {
      const f32x4 tm = row(0);
      const float M = max4(tm);
#pragma unroll
      for (int w = 0; w < WAVES; ++w) fw[w] = __builtin_amdgcn_exp2f(tm[w] - M);
      asm volatile("" : "+v"(fw));
    }
    int po = lane < 32 ? wave * 4096 + lane * 128 : 1 << 21;
    int oo = tid & 1 ? OREC : (split - 1) * OS + tid * 8;
    asm volatile("" : "+v"(P1), "+v"(P2), "+v"(po), "+v"(oo), "+s"(ro));
    pace(tgt, TAU);
    pull(rg, po, split * SPB + CL * CH);
    chunk(IC<1>(), IC<8>(), IC<0>(), P1, rg, 0, 0);
    chunk(IC<0>(), IC<4>(), IC<0>(), P2, rg, 0, 0);
    unload();
    sync();
    float on = fw[0] * lds[LDS_O + tid];
#pragma unroll
    for (int w = 1; w < WAVES; ++w) on = fmaf(fw[w], lds[LDS_O + w * 256 + tid], on);
    post(tag, on, dpp<XOR1>(on), ro, oo);
    asm volatile("s_waitcnt vmcnt(0)" ::: "memory");
    return;
  }

  look<4>(rs, soff, SS);
  pair(10, IC<10>());
  unsigned P0, P1, P2;
  {
    f32x4 x[3];
    take<4, 8>(x[0], x[1], x[2], tag, rs, soff, SS);
    constexpr int EVEN = 0xA0, ODD = 0xF5;
    float ms[3], m2 = mc;
#pragma unroll
    for (int s = 0; s < 3; ++s) { ms[s] = dpp<EVEN>(x[s][1]); m2 = fmaxf(m2, ms[s]); }
    const float f = __builtin_amdgcn_exp2f(mc - m2), fo = dpp<XOR2>(f);
    const float f0 = hl ? fo : f, f1 = hl ? f : fo;
    mc = m2; l *= f;
    acc[0] *= f0; acc[1] *= f0; acc[2] *= f1; acc[3] *= f1;
    asm volatile("" : "+a"(acc));
    const unsigned y6 = both(wexp(sc[6])), y7 = both(wexp(sc[7]));
    P0 = same(y6); P1 = swap(y6); P2 = same(y7);
    unsigned* sX = sW + 8 * 64 - (lane & 15) + (lane & 3) * 4 + ((lane >> 2) & 3);
    sX[0] = swap(y7);
#pragma unroll
    for (int s = 0; s < 3; ++s) {
      const float fs = __builtin_amdgcn_exp2f(ms[s] - m2);
      const float h0 = x[s][1], h1 = x[s][3];
      const unsigned pp = __float_as_uint(dpp<ODD>(b2 ? h1 : h0));
      const float p = __uint_as_float(hl ? pp & 0xffff0000u : pp << 16) * fs;
      sX[(1 + s) * 64] = same(pack(p, p));
      l = fmaf(h1, fs, l);
    }
    if ((lane & 61) == 0) { sM[0] = mc; sM[8] = l; sM[16] = ms[0]; sM[24] = ms[1]; sM[32] = ms[2]; }
    each(IC<4>(), [&](auto i) { fetchw<i>(to - c * 4 + (lane & 3) * 16); });
  }
  chunk(IC<0>(), IC<7>(), IC<1>(), P0, rg, 14 * CH, TAU);
  {
    u32x4 bm;
#pragma unroll
    for (int i = 0; i < 4; ++i) bm[i] = ((lane >> 2) & 3) == i ? ~0u : 0u;
    each(IC<4>(), [&](auto i) { spread<i>(bm); });
  }
  chunk(IC<1>(), IC<7>(), IC<1>(), P1, rg, tofs(0), TAU);
  sync();
  if (lane == 0) asm volatile("global_store_dword %0, %1, %2 sc1" :: "v"(0), "v"(tag), "s"(ep) : "memory");
  float cs[3];
  int oo = tid * 8;
  {
    const f32x4 tm = row(0), tl = row(1);
    const float M = max4(tm);
    float L = 0.f;
#pragma unroll
    for (int w = 0; w < WAVES; ++w) { fw[w] = __builtin_amdgcn_exp2f(tm[w] - M); L = fmaf(fw[w], tl[w], L); }
    const float r = __builtin_amdgcn_rcpf(L);
    fw *= r;
#pragma unroll
    for (int s = 0; s < 3; ++s) cs[s] = __builtin_amdgcn_exp2f(max4(row(2 + s)) - M) * r;
    asm volatile("" : "+v"(fw), "+v"(cs[0]), "+v"(cs[1]), "+v"(cs[2]), "+v"(oo), "+s"(ro));
  }
  chunk(IC<0>(), IC<8>(), IC<1>(), P2, rg, tofs(4), DEL);
  each(IC<8>(), [&](auto u) { tail_load<8 + u>(rg, voff); });
  each(IC<8>(), [&](auto k) { tail_mul<k, 15 - k + (k < 4)>(acc); });
  look<2>(ro, oo, OS);
  each(IC<8>(), [&](auto k) { tail_mul<8 + k, 10 - k>(acc); });
  asm volatile("s_nop 7\n\ts_nop 1" : "+a"(acc));
  unload();
  float y = 0.f;
  {
    f32x4 x[3];
    take<2, 0>(x[0], x[1], x[2], tag, ro, oo, OS);
#pragma unroll
    for (int s = 0; s < 3; ++s) y = fmaf(cs[s], x[s][1], y);
    asm volatile("" : "+v"(y));
  }
  sync();
#pragma unroll
  for (int w = 0; w < WAVES; ++w) y = fmaf(fw[w], lds[LDS_O + w * 256 + tid], y);
  out[grp * 256 + tid] = __builtin_bit_cast(unsigned short, (__bf16)y);
}

extern "C" void kernel_launch(void* const* d_in, const int* in_sizes, int n_in,
                              void* d_out, int out_size, void* d_ws, size_t ws_size,
                              hipStream_t stream) {
  (void)in_sizes; (void)n_in; (void)out_size; (void)ws_size;
  gqa_decode_kernel<<<NGRP * NSPLIT, NTHR, 0, stream>>>((const u32x4*)d_in[0], (const unsigned char*)d_in[1], (const unsigned char*)d_in[2],
                                                        (unsigned char*)d_ws, (unsigned short*)d_out);
}
```

```cpp
#include <hip/hip_runtime.h>
#include <hip/hip_bf16.h>
#include <stdint.h>
#include <type_traits>
#include <utility>

typedef __attribute__((ext_vector_type(4))) unsigned u32x4;
typedef __attribute__((ext_vector_type(2))) unsigned u32x2;
typedef __attribute__((ext_vector_type(4))) float f32x4;
typedef __attribute__((ext_vector_type(2))) float f32x2;
typedef __attribute__((ext_vector_type(8))) __bf16 bf16x8;
typedef __attribute__((ext_vector_type(2))) __bf16 bf16x2;
typedef __attribute__((ext_vector_type(4))) int rsrc_t;

constexpr int NB = 4, HKV = 16, SK = 4096, D = 128;
constexpr int NGRP = NB * HKV;
constexpr int NSPLIT = 4, WAVES = 4, NTHR = WAVES * 64;
constexpr int KPS = SK / NSPLIT;
constexpr int SPB = KPS * D * 2;
constexpr int CH = 16384;
constexpr int NCH = SPB / CH;
constexpr int CL = NCH - 1;
constexpr int NST = 24;
constexpr float C2 = 0.08838834764831845f * 1.4426950408889634f;

constexpr unsigned TAU = (unsigned)(NGRP * NSPLIT * 16384.0 / 7700e9 * 1e8 * 256);
constexpr unsigned LEAD = 25, FIRST_DATA = 5;
constexpr unsigned DEL = 30 << 8;
constexpr int CODE_LINES = 128;

constexpr size_t WS_S = 512u << 10, WS_O = 1u << 20;
constexpr int SW = 192, SS = WAVES * SW, SREC = 3 * SS, OS = 2048, OREC = 3 * OS;

__device__ __forceinline__ float dot2(unsigned a, unsigned b, float acc) {
  return __builtin_amdgcn_fdot2_f32_bf16(__builtin_bit_cast(bf16x2, a), __builtin_bit_cast(bf16x2, b), acc, false);
}
template <int CTRL> __device__ __forceinline__ float dpp(float x) {
  return __builtin_bit_cast(float, __builtin_amdgcn_mov_dpp(__builtin_bit_cast(int, x), CTRL, 0xf, 0xf, true));
}
constexpr int XOR1 = 0xB1, XOR2 = 0x4E, ROR4 = 0x124, ROR8 = 0x128, ROR12 = 0x12C, IDENT = 0xE4;
#define STEP(OP, HOW) "s_nop 1\n\t" OP "_dpp %0, %0, %0 " HOW " row_mask:0xf bank_mask:0xf\n\t"
#define SWAP(OP, N) "v_mov_b32 %1, %0\n\ts_nop 1\n\tv_permlane" N "_swap_b32 %0, %1\n\t" OP " %0, %0, %1\n\t"
#define ACROSS(NAME, OP) \
  __device__ __forceinline__ float NAME(float x) { \
    float y; \
    asm(STEP(OP, "quad_perm:[1,0,3,2]") STEP(OP, "row_ror:4") STEP(OP, "row_ror:8") SWAP(OP, "16") SWAP(OP, "32") : "+v"(x), "=&v"(y)); \
    return x; }
ACROSS(head_max, "v_max_f32") ACROSS(head_sum, "v_add_f32")
__device__ __forceinline__ rsrc_t mkrsrc(const void* p, int bytes) {
  const unsigned long long a = (unsigned long long)p;
  return rsrc_t{(int)(unsigned)a, (int)(unsigned)(a >> 32) & 0xffff, bytes, 0x00020000};
}
template <int U> __device__ __forceinline__ void ldstream(rsrc_t r, int voff, int soff);
template <int U, int N> struct Landed;
#define RING(U, A, B, C, D) \
  template <> __device__ __forceinline__ void ldstream<U>(rsrc_t r, int voff, int soff) { \
    asm volatile("buffer_load_dwordx4 v[" #A ":" #D "], %0, %1, %2 offen nt" :: "v"(voff), "s"(r), "s"(soff) : "memory", "v" #A, "v" #B, "v" #C, "v" #D); \
    __builtin_amdgcn_sched_barrier(0); } \
  template <int N> struct Landed<U, N> { static __device__ __forceinline__ u32x4 go() { \
    u32x4 v; \
    asm volatile("s_waitcnt vmcnt(%1)" : "={v[" #A ":" #D "]}"(v) : "n"(N) : "memory"); \
    __builtin_amdgcn_sched_barrier(0); \
    return v; } };
RING(0, 64, 65, 66, 67) RING(1, 68, 69, 70, 71) RING(2, 72, 73, 74, 75) RING(3, 76, 77, 78, 79)
RING(4, 80, 81, 82, 83) RING(5, 84, 85, 86, 87) RING(6, 88, 89, 90, 91) RING(7, 92, 93, 94, 95)
__device__ __forceinline__ void pace(unsigned& tgt, unsigned dt) {
  asm volatile("s_add_u32 %0, %0, %1\n\t"
               "s_branch 1f\n"
               "0:\n\t"
               "s_sleep 1\n"
               "1:\n\t"
               "s_memrealtime s[96:97]\n\t"
               "s_waitcnt lgkmcnt(0)\n\t"
               "s_lshl_b32 s96, s96, 8\n\t"
               "s_sub_u32 s96, s96, %0\n\t"
               "s_cmp_lt_i32 s96, 0\n\t"
               "s_cbranch_scc1 0b"
               : "+s"(tgt) : "s"(dt) : "s96", "s97", "scc", "memory");
  __builtin_amdgcn_sched_barrier(0);
}
template <int R, int B> __device__ __forceinline__ unsigned place(unsigned old, unsigned src) {
  constexpr int n = (R - B) & 3;
  constexpr int ctrl = n == 0 ? IDENT : n == 1 ? ROR4 : n == 2 ? ROR8 : ROR12;
  return (unsigned)__builtin_amdgcn_update_dpp((int)old, (int)src, ctrl, 0xf, 1 << R, false);
}
template <int B> __device__ __forceinline__ void place4(u32x4& a, unsigned src) {
  a[0] = place<0, B>(a[0], src); a[1] = place<1, B>(a[1], src); a[2] = place<2, B>(a[2], src); a[3] = place<3, B>(a[3], src);
}
__device__ __forceinline__ f32x4 mfma(u32x4 a, u32x4 b, f32x4 c) {
  c = __builtin_amdgcn_mfma_f32_16x16x32_bf16(__builtin_bit_cast(bf16x8, a), __builtin_bit_cast(bf16x8, b), c, 0, 0, 0);
  asm volatile("" : "+a"(c));
  __builtin_amdgcn_sched_barrier(0);
  return c;
}
#define LOOK(N, A, B, C) "buffer_load_dwordx" #N " v[100:" #A "], %[vo], %[rp], 0 offen sc1\n\t" \
                         "buffer_load_dwordx" #N " v[104:" #B "], %[vo], %[rp], %[s2] offen sc1\n\t" \
                         "buffer_load_dwordx" #N " v[108:" #C "], %[vo], %[rp], %[s3] offen sc1\n\t"
#define LOOK4 LOOK(4, 103, 107, 111)
#define LOOK2 LOOK(2, 101, 105, 109)
#define LOOK_IN [vo] "v"(vo), [rp] "s"(r), [s2] "s"(step), [s3] "s"(2 * step)
#define LOOK_REGS "v100", "v101", "v102", "v103", "v104", "v105", "v106", "v107", "v108", "v109", "v110", "v111"
template <int N> __device__ __forceinline__ void look(rsrc_t r, int vo, int step) {
  if (N == 4) asm volatile(LOOK4 :: LOOK_IN : "memory", LOOK_REGS);
  else asm volatile(LOOK2 :: LOOK_IN : "memory", LOOK_REGS);
  __builtin_amdgcn_sched_barrier(0);
}
__device__ __forceinline__ void pull(rsrc_t r, int vo, int so) {
  asm volatile("buffer_load_dword v100, %0, %1, %2 offen sc0 sc1" :: "v"(vo), "s"(r), "s"(so) : "memory", "v100");
  __builtin_amdgcn_sched_barrier(0);
}
__device__ __forceinline__ void post(unsigned tag, float a, float b, rsrc_t r, int vo) {
  const u32x4 rec = {tag, __float_as_uint(a), tag, __float_as_uint(b)};
  asm volatile("buffer_store_dwordx4 %0, %1, %2, 0 offen sc1\n\ts_nop 1" :: "v"(rec), "v"(vo), "s"(r) : "memory");
}
#define FIRST(A, B) "v_bitop3_b32 %[d], v" #A ", v" #B ", %[tag] bitop3:0x7e\n\t"
#define ALSO(A) "v_bitop3_b32 %[d], %[d], v" #A ", %[tag] bitop3:0xf6\n\t"
#define TAKE(CHECK, AGAIN) \
  asm volatile("s_waitcnt vmcnt(%[w])\n" \
               "0:\n\t" CHECK \
               "v_cmp_ne_u32 vcc, 0, %[d]\n\t" \
               "s_cbranch_vccz 1f\n\t" \
               "s_sub_u32 %[n], %[n], 1\n\t" \
               "s_cbranch_scc1 1f\n\t" AGAIN \
               "s_waitcnt vmcnt(0)\n\t" \
               "s_branch 0b\n" \
               "1:" \
               : "=&{v[100:103]}"(a), "=&{v[104:107]}"(b), "=&{v[108:111]}"(c), [d] "=&v"(d), [n] "+s"(spin) \
               : [tag] "s"(tag), [w] "n"(W), LOOK_IN : "memory", "scc", "vcc")
template <int N, int W> __device__ __forceinline__ void take(f32x4& a, f32x4& b, f32x4& c, unsigned tag, rsrc_t r, int vo, int step) {
  unsigned d, spin = 1u << 18;
  if (N == 4) TAKE(FIRST(100, 102) ALSO(104) ALSO(106) ALSO(108) ALSO(110), LOOK4);
  else TAKE(FIRST(100, 104) ALSO(108), LOOK2);
}
constexpr int tofs(int n) { return n / 4 * SPB + CL * CH + (n & 3) * 4096; }
template <int I> __device__ __forceinline__ void fetchw(unsigned a) {
  asm volatile("ds_read_b128 v[%c1:%c2], %0 offset:%c3" :: "v"(a), "n"(140 + I * 16), "n"(143 + I * 16), "n"((16 + I) * 256) : "memory", "v191", "a47");
}
#define AND4(D, S) "v_and_b32 v[%c4+" #D "], v[%c4+" #S "], %0\n\tv_and_b32 v[%c4+" #D "+1], v[%c4+" #S "], %1\n\t" \
                   "v_and_b32 v[%c4+" #D "+2], v[%c4+" #S "], %2\n\tv_and_b32 v[%c4+" #D "+3], v[%c4+" #S "], %3\n\t"
template <int I> __device__ __forceinline__ void spread(u32x4 m) {
  asm volatile(AND4(0, 12) AND4(4, 13) AND4(8, 14) AND4(12, 15) :: "v"(m[0]), "v"(m[1]), "v"(m[2]), "v"(m[3]), "n"(128 + I * 16) : "memory");
}
template <int N> __device__ __forceinline__ void tail_load(rsrc_t r, int voff) {
  asm volatile("buffer_load_dwordx4 a[%c3:%c3+3], %0, %1, %2 offen nt" :: "v"(voff), "s"(r), "s"(tofs(N)), "n"(N * 4 - 16) : "memory");
  __builtin_amdgcn_sched_barrier(0);
}
template <int N, int W> __device__ __forceinline__ void tail_mul(f32x4& acc) {
  constexpr int x = N < 8 ? 64 + (N ^ 4) * 4 : N * 4 - 16, y = 128 + N * 4;
  if (N & 8) asm volatile("s_waitcnt vmcnt(%c3)\n\tv_mfma_f32_16x16x32_bf16 %0, v[%c1:%c1+3], a[%c2:%c2+3], %0" : "+a"(acc) : "n"(y), "n"(x), "n"(W) : "memory");
  else asm volatile("s_waitcnt vmcnt(%c3)\n\tv_mfma_f32_16x16x32_bf16 %0, v[%c1:%c1+3], v[%c2:%c2+3], %0" : "+a"(acc) : "n"(y), "n"(x), "n"(W) : "memory");
  __builtin_amdgcn_sched_barrier(0);
}

constexpr int LDS_T = 0;
constexpr int LDS_O = WAVES * NST * 64;
constexpr int LDS_M = LDS_O + WAVES * 256;
constexpr int LDS_N = 21 * 1024;
template <int I> using IC = std::integral_constant<int, I>;

__global__ void __launch_bounds__(NTHR)
gqa_decode_kernel(const u32x4* __restrict__ q, const unsigned char* kc, const unsigned char* vc,
                  unsigned short* __restrict__ out, unsigned char* __restrict__ ws) {
#pragma clang fp contract(off)
  __shared__ __attribute__((aligned(16))) float lds[LDS_N];
  const int tid = threadIdx.x, lane = tid & 63;
  const int wave = __builtin_amdgcn_readfirstlane(tid >> 6);
  const int lin = (blockIdx.x & 7) * (NGRP * NSPLIT / 8) + (blockIdx.x >> 3);
  const int grp = lin / NSPLIT, split = lin % NSPLIT;
  const int c = lane & 15, hl = (lane >> 1) & 1;
  const bool b3 = lane & 8, b2 = lane & 4, odd = lane & 1;

  int voff = tid * 16, qo = hl * 256 + c * 16, qx = qo ^ 256;
  int to = (LDS_T + wave * NST * 64 + lane) * 4, so = (LDS_O + wave * 256 + c * 8 + (lane >> 4) * 2) * 4;
  int soff = wave * SW + (odd ? (lane >> 3) * 16 : 128 + hl * 16);
  int woff = (lane & (odd ? 2 : 64 - (SW - 128) / 4)) * SREC + soff + (lane & 4) * 2 + (odd ? 0 : (lane >> 3) * 32) + ((split - 1) & 3) * SS;
  unsigned keep = odd ? 0xffff0000u : 0x0000ffffu, flip = odd ? 0x01000c0cu : 0x0c0c0302u;
  int c0 = min((int)(blockIdx.x >> 3) * 4 + wave, CODE_LINES - 1) * 64;
  asm volatile("" : "+v"(voff), "+v"(qo), "+v"(qx), "+v"(to), "+v"(so), "+v"(soff), "+v"(woff), "+v"(keep), "+v"(flip), "+s"(c0));
  __builtin_amdgcn_sched_barrier(0);

  const unsigned char* kp = kc + (size_t)lin * SPB;
  const unsigned char* vp = vc + (size_t)lin * SPB;
  u32x4 qa, qb;
  asm volatile("buffer_load_dwordx4 %0, %2, %4, 0 offen nt\n\tbuffer_load_dwordx4 %1, %3, %4, 0 offen nt"
               : "=&v"(qa), "=&v"(qb) : "v"(qo), "v"(qx), "s"(mkrsrc(q + grp * 32, 512)) : "memory");
  auto each = [](auto n, auto f) { [&]<int... U>(std::integer_sequence<int, U...>) { (f(IC<U>()), ...); }(std::make_integer_sequence<int, decltype(n)::value>()); };
  {
    const rsrc_t r = mkrsrc(kp, 2 * CH);
    each(IC<8>(), [&](auto u) { ldstream<u>(r, voff, u * 4096); });
  }
  unsigned* ep = reinterpret_cast<unsigned*>(ws + (size_t)grp * 128);
  rsrc_t rs, ro;
  unsigned tag, tgt;
  {
    unsigned long long t0, t1;
    asm volatile("s_memrealtime %1\n\t"
                 "s_load_dword %0, %2, 0\n\t"
                 "s_load_dword s96, %3, 0\n\t"
                 "s_getpc_b64 s[94:95]\n\t"
                 "s_load_dword s97, s[94:95], %4"
                 : "=&s"(tag), "=&s"(t0) : "s"(ep), "s"(vp), "s"(c0) : "memory", "s94", "s95", "s96", "s97");
    __builtin_amdgcn_sched_barrier(0);
    rs = mkrsrc(ws + WS_S + grp * SREC, SREC);
    ro = mkrsrc(ws + WS_O + grp * OREC, OREC);
    asm volatile("s_waitcnt vmcnt(7)\n\t"
                 "s_memrealtime %2\n\t"
                 "s_waitcnt lgkmcnt(0)\n\t"
                 "s_add_u32 %0, %0, 0x9e3779b9"
                 : "+s"(tag), "+s"(t0), "=&s"(t1), "+v"(qa), "+v"(qb), "+s"(rs), "+s"(ro)
                 :: "memory", "scc", "s96", "s97");
    tgt = ((unsigned)t0 + LEAD) << 8;
    const unsigned t = (((unsigned)t1 + FIRST_DATA) << 8) - TAU;
    if ((int)(t - tgt) > 0) tgt = t;
    __builtin_amdgcn_sched_barrier(0);
  }
  __builtin_assume((so & 7) == 0);
  float* sT = reinterpret_cast<float*>(reinterpret_cast<char*>(lds) + to);
  float mx = -INFINITY;
#pragma unroll 1
  for (int it = 0; it < NCH / 2; ++it) {
    const rsrc_t r = mkrsrc(it + 1 < NCH / 2 ? kp + (size_t)(it + 1) * 2 * CH : vp, 2 * CH);
    float tt[2];
    each(IC<2>(), [&](auto a) {
      float z[4];
      each(IC<4>(), [&](auto b) {
        constexpr int u = a * 4 + b;
        float da = 0.f, db = 0.f;
        const u32x4 kv = Landed<u, 7>::go();
#pragma unroll
        for (int k = 0; k < 4; ++k) { da = dot2(kv[k], qa[k], da); db = dot2(kv[k], qb[k], db); }
        asm volatile("" : "+v"(da), "+v"(db));
        __builtin_amdgcn_sched_barrier(0);
        if (b == 0) pace(tgt, TAU);
        ldstream<u>(r, voff, u * 4096);
        z[b] = da + dpp<XOR2>(db);
      });
      const float u0 = z[0] + dpp<ROR8>(z[0]), u2 = z[2] + dpp<ROR8>(z[2]);
      const float u1 = z[1] + dpp<ROR8>(z[1]), u3 = z[3] + dpp<ROR8>(z[3]);
      const float w0 = b3 ? u2 : u0, w1 = b3 ? u3 : u1;
      const float x0 = w0 + dpp<0x104>(w0);
      const float x1 = w1 + dpp<0x114>(w1);
      tt[a] = b2 ? x1 : x0;
      asm volatile("" : "+v"(tt[a]));
      __builtin_amdgcn_sched_barrier(0);
    });
    const float t0 = tt[0], t1 = tt[1];
    const float t = (odd ? t1 : t0) + dpp<XOR1>(odd ? t0 : t1);
    mx = fmaxf(mx, t);
    sT[it * 64] = t;
    __builtin_amdgcn_sched_barrier(0);
  }
  float sc[8];
#pragma unroll
  for (int i = 0; i < 8; ++i) sc[i] = sT[i * 64];
  __builtin_amdgcn_sched_barrier(0);
  float mc = head_max(mx) * C2;
  auto pack = [](float a, float b) { bf16x2 v; v[0] = (__bf16)a; v[1] = (__bf16)b; return __builtin_bit_cast(unsigned, v); };
  auto wexp = [&](float t) { return __builtin_amdgcn_exp2f(fmaf(t, C2, -mc)); };
  auto both = [&](float p) { return pack(p, dpp<XOR1>(p)); };
  auto same = [&](unsigned y) { return y & keep; };
  auto swap = [&](unsigned y) { return __builtin_amdgcn_perm(y, y, flip); };
  unsigned* sW = reinterpret_cast<unsigned*>(sT) + 8 * 64;
  float l = 0.f;
  {
#pragma unroll
    for (int i = 0; i < 7; ++i) { const float p = wexp(sc[i]); l += p; sW[i * 64] = both(p); }
    const float p = wexp(sc[7]);
    sW[7 * 64] = both(p);
    const unsigned w = pack(p, dpp<XOR2>(p));
    l = head_sum(l + p);
    const u32x2 rec = {tag, odd ? w : __float_as_uint(b2 ? l : mc)};
    asm volatile("buffer_store_dwordx2 %0, %1, %2, 0 offen sc1" :: "v"(rec), "v"(woff), "s"(rs) : "memory");
    __builtin_amdgcn_sched_barrier(0);
  }
  f32x4 acc = {0.f, 0.f, 0.f, 0.f};
  u32x4 A0 = {0u, 0u, 0u, 0u}, A1 = {0u, 0u, 0u, 0u};
  auto chunk = [&](auto h, auto w, auto next, unsigned P, rsrc_t r, int so, unsigned dt) {
    each(IC<4>(), [&](auto b) {
      constexpr int u = h * 4 + b;
      u32x4& A = (b & 1) ? A1 : A0;
      place4<b>(A, P);
      asm volatile("" : "+v"(A));
      acc = mfma(A, Landed<u, next ? w : w - b>::go(), acc);
      if (next && b == 0) pace(tgt, dt);
      if (next) ldstream<u>(r, voff, so + b * 4096);
    });
  };
  auto pair = [&](int g, auto w) {
    const rsrc_t r = mkrsrc(vp + (size_t)(g + 2) * CH, 2 * CH);
    const unsigned y = sW[g * 32];
    chunk(IC<0>(), w, IC<1>(), same(y), r, 0, TAU);
    chunk(IC<1>(), w, IC<1>(), swap(y), r, CH, TAU);
  };
  auto unload = [&] {
    *reinterpret_cast<f32x2*>((char*)lds + so) = f32x2{acc[0], acc[1]};
    *reinterpret_cast<f32x2*>((char*)lds + so + 512) = f32x2{acc[2], acc[3]};
    __builtin_amdgcn_sched_barrier(0);
  };
  auto sync = [] { asm volatile("s_waitcnt lgkmcnt(0)\n\ts_barrier" ::: "memory"); };
  float* sM = lds + LDS_M + hl * 4 + wave;
  auto row = [&](int i) { return *reinterpret_cast<const f32x4*>(lds + LDS_M + i * 8 + (tid >> 7) * 4); };
  auto max4 = [](f32x4 m) { return fmaxf(fmaxf(m[0], m[1]), fmaxf(m[2], m[3])); };
  f32x4 fw;
  pair(0, IC<8>());
#pragma unroll 1
  for (int g = 2; g < 10; g += 2) pair(g, IC<7>());
  const rsrc_t rg = mkrsrc(vc + (size_t)grp * (NSPLIT * SPB), NSPLIT * SPB);

  if (split) {
    if ((lane & 61) == 0) sM[0] = mc;
    pair(10, IC<7>());
    const unsigned y6 = sW[6 * 64];
    unsigned P1 = swap(y6), P2 = same(sW[7 * 64]);
    chunk(IC<0>(), IC<7>(), IC<1>(), same(y6), rg, split * SPB + 14 * CH, TAU);
    sync();
    {
      const f32x4 tm = row(0);
      const float M = max4(tm);
#pragma unroll
      for (int w = 0; w < WAVES; ++w) fw[w] = __builtin_amdgcn_exp2f(tm[w] - M);
      asm volatile("" : "+v"(fw));
    }
    int po = lane < 32 ? wave * 4096 + lane * 128 : 1 << 21;
    int oo = tid & 1 ? OREC : (split - 1) * OS + tid * 8;
    asm volatile("" : "+v"(P1), "+v"(P2), "+v"(po), "+v"(oo), "+s"(ro));
    pace(tgt, TAU);
    pull(rg, po, split * SPB + CL * CH);
    chunk(IC<1>(), IC<8>(), IC<0>(), P1, rg, 0, 0);
    chunk(IC<0>(), IC<4>(), IC<0>(), P2, rg, 0, 0);
    unload();
    sync();
    float on = fw[0] * lds[LDS_O + tid];
#pragma unroll
    for (int w = 1; w < WAVES; ++w) on = fmaf(fw[w], lds[LDS_O + w * 256 + tid], on);
    post(tag, on, dpp<XOR1>(on), ro, oo);
    asm volatile("s_waitcnt vmcnt(0)" ::: "memory");
    return;
  }

  look<4>(rs, soff, SS);
  pair(10, IC<10>());
  unsigned P0, P1, P2;
  {
    f32x4 x[3];
    take<4, 8>(x[0], x[1], x[2], tag, rs, soff, SS);
    constexpr int EVEN = 0xA0, ODD = 0xF5;
    float ms[3], m2 = mc;
#pragma unroll
    for (int s = 0; s < 3; ++s) { ms[s] = dpp<EVEN>(x[s][1]); m2 = fmaxf(m2, ms[s]); }
    const float f = __builtin_amdgcn_exp2f(mc - m2), fo = dpp<XOR2>(f);
    const float f0 = hl ? fo : f, f1 = hl ? f : fo;
    mc = m2; l *= f;
    acc[0] *= f0; acc[1] *= f0; acc[2] *= f1; acc[3] *= f1;
    asm volatile("" : "+a"(acc));
    const unsigned y6 = both(wexp(sc[6])), y7 = both(wexp(sc[7]));
    P0 = same(y6); P1 = swap(y6); P2 = same(y7);
    unsigned* sX = sW + 8 * 64 - (lane & 15) + (lane & 3) * 4 + ((lane >> 2) & 3);
    sX[0] = swap(y7);
#pragma unroll
    for (int s = 0; s < 3; ++s) {
      const float fs = __builtin_amdgcn_exp2f(ms[s] - m2);
      const float h0 = x[s][1], h1 = x[s][3];
      const unsigned pp = __float_as_uint(dpp<ODD>(b2 ? h1 : h0));
      const float p = __uint_as_float(hl ? pp & 0xffff0000u : pp << 16) * fs;
      sX[(1 + s) * 64] = same(pack(p, p));
      l = fmaf(h1, fs, l);
    }
    if ((lane & 61) == 0) { sM[0] = mc; sM[8] = l; sM[16] = ms[0]; sM[24] = ms[1]; sM[32] = ms[2]; }
    each(IC<4>(), [&](auto i) { fetchw<i>(to - c * 4 + (lane & 3) * 16); });
  }
  chunk(IC<0>(), IC<7>(), IC<1>(), P0, rg, 14 * CH, TAU);
  {
    u32x4 bm;
#pragma unroll
    for (int i = 0; i < 4; ++i) bm[i] = ((lane >> 2) & 3) == i ? ~0u : 0u;
    each(IC<4>(), [&](auto i) { spread<i>(bm); });
  }
  chunk(IC<1>(), IC<7>(), IC<1>(), P1, rg, tofs(0), TAU);
  sync();
  if (lane == 0) asm volatile("global_store_dword %0, %1, %2 sc1" :: "v"(0), "v"(tag), "s"(ep) : "memory");
  float cs[3];
  int oo = tid * 8;
  {
    const f32x4 tm = row(0), tl = row(1);
    const float M = max4(tm);
    float L = 0.f;
#pragma unroll
    for (int w = 0; w < WAVES; ++w) { fw[w] = __builtin_amdgcn_exp2f(tm[w] - M); L = fmaf(fw[w], tl[w], L); }
    const float r = __builtin_amdgcn_rcpf(L);
    fw *= r;
#pragma unroll
    for (int s = 0; s < 3; ++s) cs[s] = __builtin_amdgcn_exp2f(max4(row(2 + s)) - M) * r;
    asm volatile("" : "+v"(fw), "+v"(cs[0]), "+v"(cs[1]), "+v"(cs[2]), "+v"(oo), "+s"(ro));
  }
  chunk(IC<0>(), IC<8>(), IC<1>(), P2, rg, tofs(4), DEL);
  each(IC<8>(), [&](auto u) { tail_load<8 + u>(rg, voff); });
  each(IC<8>(), [&](auto k) { tail_mul<k, 15 - k + (k < 4)>(acc); });
  look<2>(ro, oo, OS);
  each(IC<8>(), [&](auto k) { tail_mul<8 + k, 10 - k>(acc); });
  asm volatile("s_nop 7\n\ts_nop 1" : "+a"(acc));
  unload();
  float y = 0.f;
  {
    f32x4 x[3];
    take<2, 0>(x[0], x[1], x[2], tag, ro, oo, OS);
#pragma unroll
    for (int s = 0; s < 3; ++s) y = fmaf(cs[s], x[s][1], y);
    asm volatile("" : "+v"(y));
  }
  sync();
#pragma unroll
  for (int w = 0; w < WAVES; ++w) y = fmaf(fw[w], lds[LDS_O + w * 256 + tid], y);
  out[grp * 256 + tid] = __builtin_bit_cast(unsigned short, (__bf16)y);
}

extern "C" void kernel_launch(void* const* d_in, const int* in_sizes, int n_in,
                              void* d_out, int out_size, void* d_ws, size_t ws_size,
                              hipStream_t stream) {
  (void)in_sizes; (void)n_in; (void)out_size; (void)ws_size;
  gqa_decode_kernel<<<NGRP * NSPLIT, NTHR, 0, stream>>>((const u32x4*)d_in[0], (const unsigned char*)d_in[1], (const unsigned char*)d_in[2],
                                                        (unsigned short*)d_out, (unsigned char*)d_ws);
}
```
